```python
import torch

def _softmax(x, axis=-1):
    return torch.softmax(x, dim=axis)
def _gelu(x):
    return torch.nn.functional.gelu(x, approximate="tanh")
def _silu(x):
    return torch.nn.functional.silu(x)
def _sigmoid(x):
    return torch.sigmoid(x)
def _layernorm(x, axis=-1, eps=1e-5):
    m = x.mean(dim=axis, keepdim=True)
    v = x.var(dim=axis, keepdim=True, unbiased=False)
    return (x - m) / torch.sqrt(v + eps)
def _scatter_add(tgt, idx, src, axis=0):
    return tgt.clone().index_add_(axis, idx, src)
def _segment_sum(src, seg, n):
    out = torch.zeros((n,) + src.shape[1:], dtype=src.dtype)
    return out.index_add_(0, seg, src)
def _pad(x, pw):
    flat = tuple(v for pair in reversed(pw) for v in pair)
    return torch.nn.functional.pad(x, flat)

kernel_name = 'standalone__dense_activation_relu__prompt'

def setup_inputs():

    _w = (lambda a: (lambda t: t.to(torch.bfloat16) if t.is_floating_point() else t)(torch.as_tensor(a)))
    B, S, D, Dff = 1, 2048, 640, 1728
    return {
        "x": _w((lambda *s: torch.randn(*s, dtype=torch.bfloat16) * 0.05)(B, S, D)),
        "w1": _w((lambda *s: torch.randn(*s, dtype=torch.bfloat16) * 0.05)(D, Dff)),
        "w2": _w((lambda *s: torch.randn(*s, dtype=torch.bfloat16) * 0.05)(Dff, D)),
    }

def reference(x, w1, w2):
    h = torch.einsum("bsd,df->bsf", x, w1)
    zero = torch.zeros_like(h)
    h = torch.maximum(h, zero)
    return torch.einsum("bsf,fd->bsd", h, w2)
```

```cpp
#include <hip/hip_runtime.h>
#include <stdint.h>

typedef short bf16x8 __attribute__((ext_vector_type(8)));
typedef short s16x4 __attribute__((ext_vector_type(4)));
typedef float f32x4 __attribute__((ext_vector_type(4)));
typedef float f32x2 __attribute__((ext_vector_type(2)));
typedef float f32x16 __attribute__((ext_vector_type(16)));
typedef unsigned u32x4 __attribute__((ext_vector_type(4)));
typedef __bf16 bf16x2_t __attribute__((ext_vector_type(2)));
#define LAS __attribute__((address_space(3)))
#define GAS __attribute__((address_space(1)))
#define DI __device__ __forceinline__

#define STAMPS 0

namespace {
constexpr int kM = 2048, kD = 640, kF = 1728;
constexpr unsigned X_BYTES = kM * kD * 2, W1_BYTES = kD * kF * 2, W2_BYTES = kF * kD * 2, OUT_BYTES = kM * kD * 2;
constexpr unsigned XROW = kD * 2, W1ROW = kF * 2, W2ROW = kD * 2;
constexpr int KS2 = kF / 32;
constexpr unsigned H_BYTES = (kM / 16) * KS2 * 1024;
constexpr unsigned SLOT = 32768, LDS_BYTES = 5 * SLOT;
constexpr size_t WS_NONCE = 8u << 20, WS_INBOX = WS_NONCE + 4096, WS_STAMP = WS_INBOX + 65536;

DI u32x4 mk_rsrc(const void* p, unsigned bytes) {
  uint64_t a = (uint64_t)p;
  u32x4 r;
  r[0] = (unsigned)a; r[1] = (unsigned)(a >> 32) & 0xffffu; r[2] = bytes; r[3] = 0x00020000u;
  return r;
}
template <int OFF>
DI void dma2(unsigned m0v, unsigned va, unsigned vb, u32x4 rs, unsigned soff) {
  asm volatile("s_mov_b32 m0, %0\n\ts_nop 0\n\t"
               "buffer_load_dwordx4 %1, %3, %4 offen offset:%5 lds\n\t"
               "buffer_load_dwordx4 %2, %3, %4 offen offset:%6 lds"
               :: "s"(m0v), "v"(va), "v"(vb), "s"(rs), "s"(soff), "i"(OFF), "i"(OFF + 1024) : "memory");
}
template <int OFF>
DI void dma1(unsigned m0v, unsigned va, u32x4 rs, unsigned soff) {
  asm volatile("s_mov_b32 m0, %0\n\ts_nop 0\n\t"
               "buffer_load_dwordx4 %1, %2, %3 offen offset:%4 lds"
               :: "s"(m0v), "v"(va), "s"(rs), "s"(soff), "i"(OFF) : "memory");
}
#define SB() __builtin_amdgcn_sched_barrier(0)
#define WAIT_VM(n) asm volatile("s_waitcnt vmcnt(" #n ")" ::: "memory")
#define WAIT_LGKM0() asm volatile("s_waitcnt lgkmcnt(0)" ::: "memory")
#define BARRIER() do { asm volatile("" ::: "memory"); __builtin_amdgcn_s_barrier(); asm volatile("" ::: "memory"); } while (0)

DI bf16x8 tr_read2(LAS char* p, int second_off) {
  s16x4 a = __builtin_amdgcn_ds_read_tr16_b64_v4i16((LAS s16x4*)p);
  s16x4 b = __builtin_amdgcn_ds_read_tr16_b64_v4i16((LAS s16x4*)(p + second_off));
  return __builtin_shufflevector(a, b, 0, 1, 2, 3, 4, 5, 6, 7);
}
DI unsigned pack_bf16(float a, float b) {
  f32x2 v = {a, b};
  return __builtin_bit_cast(unsigned, __builtin_convertvector(v, bf16x2_t));
}
DI float relu_nan(float v) { return __builtin_elementwise_maximum(v, 0.0f); }
DI unsigned ld_agent(const unsigned* p) { return __hip_atomic_load((const GAS unsigned*)p, __ATOMIC_RELAXED, __HIP_MEMORY_SCOPE_AGENT); }
DI void st_agent(unsigned* p, unsigned v) { __hip_atomic_store((GAS unsigned*)p, v, __ATOMIC_RELAXED, __HIP_MEMORY_SCOPE_AGENT); }

struct Frag1 { bf16x8 a[2], b[2]; };
}

#define STAMP(i) do {} while (0)

__global__ __launch_bounds__(256, 1) void ffn_relu_kernel(const char* __restrict__ x, const char* __restrict__ w1,
                                                          const char* __restrict__ w2, char* __restrict__ out,
                                                          char* __restrict__ ws) {
  extern __shared__ __attribute__((aligned(16))) char smem_g[];
  LAS char* smem = (LAS char*)smem_g;
  const unsigned lds0 = (unsigned)(size_t)smem;
  const int lane = threadIdx.x & 63;
  const int wave = __builtin_amdgcn_readfirstlane(threadIdx.x >> 6);
  const int b = blockIdx.x, xg = b & 7, idx = b >> 3;
  const int grp = xg >> 1, par = xg & 1;
  const int member = par * 32 + idx;
  STAMP(0);

  unsigned* nonce_w = (unsigned*)(ws + WS_NONCE) + grp * 64;
  unsigned* inbox = (unsigned*)(ws + WS_INBOX) + (size_t)grp * 64 * 64;
  unsigned in0;
  asm volatile("global_load_dword %0, %1, off sc1" : "=v"(in0) : "v"(inbox + member * 64 + lane) : "memory");

  int strip, pos, nmb, mb0;
  if (idx < 14) { strip = idx >= 7; pos = idx - 7 * strip; nmb = pos < 4 ? 5 : 4; mb0 = pos < 4 ? 5 * pos : 4 + 4 * pos; }
  else { int j = idx - 14; strip = 2 + j / 6; pos = j % 6; nmb = pos < 4 ? 5 : 6; mb0 = pos < 4 ? 5 * pos : 6 * pos - 4; }
  mb0 += grp * 32;
  const int lc2 = par * 5 + strip;

  const u32x4 rs_x = mk_rsrc(x, X_BYTES), rs_w1 = mk_rsrc(w1, W1_BYTES), rs_w2 = mk_rsrc(w2, W2_BYTES);
  unsigned v2[4];
#pragma unroll
  for (int i = 0; i < 4; ++i)
    v2[i] = (lane >> 3) * W2ROW + (((lane & 7) ^ ((((lane >> 4) & 1) | (((i >> 1) & 1) << 1)) << 1)) << 4) + i * (8 * W2ROW - 1024);
  const unsigned s2_base = (unsigned)(wave * 448) * W2ROW + lc2 * 128;
  auto stage_w2 = [&](int j, unsigned slot_off) {
    const unsigned m0a = lds0 + slot_off + wave * 8192, so = s2_base + (unsigned)j * (64 * W2ROW);
    dma2<0>(m0a, v2[0], v2[1], rs_w2, so);
    dma2<2048>(m0a, v2[2], v2[3], rs_w2, so);
    dma2<0>(m0a + 4096, v2[0], v2[1], rs_w2, so + 32 * W2ROW);
    dma2<2048>(m0a + 4096, v2[2], v2[3], rs_w2, so + 32 * W2ROW);
  };

  const bool has_p1 = (idx & 7) != 7;
  unsigned nonce, nd;
  if (has_p1) {
    const int pm = grp * 4 + (idx >> 3), pn = par * 7 + (idx & 7);
    const int col0 = pn == 13 ? kF - 128 : pn * 128;
    const int wm = wave >> 1, wf = wave & 1;
    int kst = (pn + 3 * pm) % 10;
    unsigned vx[4], vw[4];
#pragma unroll
    for (int i = 0; i < 4; ++i) {
      vx[i] = (lane >> 3) * XROW + (((lane & 7) ^ ((4 * (i & 1) + (lane >> 4)) & 7)) << 4) + i * (8 * XROW - 1024);
      vw[i] = (lane >> 4) * W1ROW + (((lane & 15) ^ ((lane >> 4) << 2)) << 4) + i * (4 * W1ROW - 1024);
    }
    const unsigned sx_base = (unsigned)(pm * 128 + wave * 32) * XROW, sw_base = (unsigned)(wave * 16) * W1ROW + col0 * 2;
    auto stage_p1 = [&](unsigned slot_off) {
      const unsigned m0x = lds0 + slot_off + wave * 4096, m0w = m0x + 16384;
      const unsigned sx = sx_base + kst * 128, sw = sw_base + (unsigned)kst * (64 * W1ROW);
      dma2<0>(m0x, vx[0], vx[1], rs_x, sx);
      dma2<2048>(m0x, vx[2], vx[3], rs_x, sx);
      dma2<0>(m0w, vw[0], vw[1], rs_w1, sw);
      dma2<2048>(m0w, vw[2], vw[3], rs_w1, sw);
      kst = kst == 9 ? 0 : kst + 1;
    };
    STAMP(1);
#pragma unroll
    for (int s = 0; s < 5; ++s) stage_p1(s * SLOT);
    STAMP(2);

    WAIT_VM(40);
    asm volatile("" : "+v"(in0));
    nonce = __builtin_amdgcn_readfirstlane(in0) + 1u;
    while (__builtin_amdgcn_ballot_w64(in0 == nonce) != 0) ++nonce;
    if (threadIdx.x == 0) st_agent(nonce_w + member, nonce);

    const int hi = lane >> 5, r31 = lane & 31;
    const unsigned xrow = wm * 64 + r31;
    const unsigned xa0 = xrow * 128 + ((hi ^ ((xrow >> 1) & 7)) << 4);
    const int fh = (lane >> 4) & 1, q = (lane & 15) >> 2, p = lane & 3;
    const unsigned wa0 = 16384 + (8 * hi + q) * 256 + (((8 * wf + 2 * fh + (p >> 1)) ^ (q << 2)) << 4) + 8 * (p & 1);
    f32x16 acc[2][2];
#pragma unroll
    for (int i = 0; i < 2; ++i)
#pragma unroll
      for (int j = 0; j < 2; ++j)
#pragma unroll
        for (int e = 0; e < 16; ++e) acc[i][j][e] = 0.f;
    Frag1 F[4];
    LAS char* xr[4]; LAS char* wr[2];
#pragma unroll
    for (int k = 0; k < 4; ++k) xr[k] = smem + (xa0 ^ (k << 5));
    wr[0] = smem + wa0; wr[1] = smem + (wa0 ^ 64);
    auto rd_x = [&](int ks) { F[ks].b[0] = *(LAS bf16x8*)xr[ks]; F[ks].b[1] = *(LAS bf16x8*)(xr[ks] + 4096); };
    auto rd_w = [&](int ks) { F[ks].a[0] = tr_read2(wr[0] + ks * 4096, 1024); F[ks].a[1] = tr_read2(wr[1] + ks * 4096, 1024); };
    auto mf = [&](int ks, int i, int j) { acc[i][j] = __builtin_amdgcn_mfma_f32_32x32x16_bf16(F[ks].a[i], F[ks].b[j], acc[i][j], 0, 0, 0); };
    auto advance = [&](int d) {
#pragma unroll
      for (int k = 0; k < 4; ++k) { xr[k] += d; asm volatile("" : "+v"(xr[k])); }
      wr[0] += d; wr[1] += d; asm volatile("" : "+v"(wr[0]), "+v"(wr[1]));
    };
#define STEP(use, ld, DMA_A, DMA_B) \
    mf(use, 0, 0); SB(); rd_x(ld); SB(); \
    mf(use, 0, 1); SB(); rd_w(ld); SB(); \
    mf(use, 1, 0); SB(); DMA_A; SB(); \
    mf(use, 1, 1); SB(); DMA_B; SB();
    WAIT_VM(32);
    BARRIER();
    STAMP(3);
    rd_x(0); rd_w(0); rd_x(1); rd_w(1); SB();
    STEP(0, 2, , );
    STEP(1, 3, , );
    unsigned fr = 0;
#pragma unroll 1
    for (int i = 1; i <= 5; ++i) {
      advance(i == 5 ? -(int)(4 * SLOT) : (int)SLOT);
      const unsigned m0x = lds0 + fr + wave * 4096, m0w = m0x + 16384;
      const unsigned sx = sx_base + kst * 128, sw = sw_base + (unsigned)kst * (64 * W1ROW);
      kst = kst == 9 ? 0 : kst + 1;
      fr += SLOT;
      WAIT_LGKM0();
      WAIT_VM(24);
      BARRIER();
      SB();
      STEP(2, 0, dma1<0>(m0x, vx[0], rs_x, sx), dma1<1024>(m0x, vx[1], rs_x, sx));
      STEP(3, 1, dma1<2048>(m0x, vx[2], rs_x, sx), dma1<3072>(m0x, vx[3], rs_x, sx));
      STEP(0, 2, dma1<0>(m0w, vw[0], rs_w1, sw), dma1<1024>(m0w, vw[1], rs_w1, sw));
      STEP(1, 3, dma1<2048>(m0w, vw[2], rs_w1, sw), dma1<3072>(m0w, vw[3], rs_w1, sw));
    }
#pragma unroll 1
    for (int j = 0; j < 4; ++j) {
      advance((int)SLOT);
      const unsigned m0a = lds0 + j * SLOT + wave * 8192, so = s2_base + (unsigned)j * (64 * W2ROW);
      WAIT_LGKM0();
      WAIT_VM(24);
      BARRIER();
      SB();
      STEP(2, 0, dma1<0>(m0a, v2[0], rs_w2, so), dma1<1024>(m0a, v2[1], rs_w2, so));
      STEP(3, 1, dma1<2048>(m0a, v2[2], rs_w2, so), dma1<3072>(m0a, v2[3], rs_w2, so));
      STEP(0, 2, dma1<0>(m0a + 4096, v2[0], rs_w2, so + 32 * W2ROW), dma1<1024>(m0a + 4096, v2[1], rs_w2, so + 32 * W2ROW));
      STEP(1, 3, dma1<2048>(m0a + 4096, v2[2], rs_w2, so + 32 * W2ROW), dma1<3072>(m0a + 4096, v2[3], rs_w2, so + 32 * W2ROW));
    }
    WAIT_LGKM0();
    BARRIER();
    SB();
    mf(2, 0, 0); mf(2, 0, 1); mf(2, 1, 0); mf(2, 1, 1);
    stage_w2(4, 4 * SLOT);
    mf(3, 0, 0); mf(3, 0, 1); mf(3, 1, 0); mf(3, 1, 1);
    STAMP(4);
    nd = ld_agent(nonce_w + lane);
    const auto rs_h = __builtin_amdgcn_make_buffer_rsrc((void*)ws, 0, H_BYTES, 0x00020000);
    if (pn != 13 || wf == 1) {
#pragma unroll
      for (int fb = 0; fb < 2; ++fb)
#pragma unroll
        for (int mb = 0; mb < 2; ++mb) {
          const unsigned ks2 = (col0 >> 5) + 2 * wf + fb;
          const unsigned mblk = pm * 8 + wm * 4 + mb * 2;
          const unsigned so = (mblk * KS2 + ks2) * 1024;
          const unsigned vo = (r31 >> 4) * (KS2 * 1024) + hi * 512 + (lane & 15) * 16;
#pragma unroll
          for (int s = 0; s < 2; ++s) {
            u32x4 v;
#pragma unroll
            for (int e = 0; e < 4; ++e)
              v[e] = pack_bf16(relu_nan(acc[fb][mb][8 * s + 2 * e]), relu_nan(acc[fb][mb][8 * s + 2 * e + 1]));
            __builtin_amdgcn_raw_buffer_store_b128(v, rs_h, vo + s * 256, so, 16);
          }
        }
    }
  } else {
#pragma unroll
    for (int s = 0; s < 5; ++s) stage_w2(s, s * SLOT);
    WAIT_VM(40);
    asm volatile("" : "+v"(in0));
    nonce = __builtin_amdgcn_readfirstlane(in0) + 1u;
    while (__builtin_amdgcn_ballot_w64(in0 == nonce) != 0) ++nonce;
    if (threadIdx.x == 0) st_agent(nonce_w + member, nonce);
    WAIT_VM(0);
    __builtin_amdgcn_s_sleep(48);
    nd = ld_agent(nonce_w + lane);
  }

  WAIT_VM(0);
  BARRIER();
  STAMP(5);
  if (wave == 0) st_agent(inbox + lane * 64 + member, nd);

  bf16x8 W[14][4];
  {
    const int g = lane >> 4, q = (lane & 15) >> 2, p = lane & 3;
    const unsigned f = ((q >> 1) & 1) | ((g & 1) << 1);
    const unsigned ra = wave * 8192 + (16 * (g & 1) + 4 * (g >> 1) + q) * 128 + ((p >> 1) << 4) + 8 * (p & 1);
    auto read_w = [&](int j, unsigned slot_off) {
#pragma unroll
      for (int k = 0; k < 2; ++k)
#pragma unroll
        for (int nb = 0; nb < 4; ++nb) W[2 * j + k][nb] = tr_read2(smem + slot_off + ra + ((nb ^ f) << 5) + k * 4096, 1024);
    };
    read_w(0, 0);
    read_w(1, SLOT);
    WAIT_LGKM0();
    stage_w2(5, 0);
    stage_w2(6, SLOT);
    read_w(2, 2 * SLOT);
    read_w(3, 3 * SLOT);
    read_w(4, 4 * SLOT);
    WAIT_VM(0);
    read_w(5, 0);
    read_w(6, SLOT);
    if (wave == 3) {
#pragma unroll
      for (int k = 12; k < 14; ++k)
#pragma unroll
        for (int nb = 0; nb < 4; ++nb) W[k][nb] = bf16x8{0, 0, 0, 0, 0, 0, 0, 0};
    }
    WAIT_LGKM0();
  }
  STAMP(6);
  if (wave == 0) {
    for (unsigned spins = 0; spins < (1u << 22); ++spins) {
      if (__builtin_amdgcn_ballot_w64(ld_agent(inbox + member * 64 + lane) != nonce) == 0) break;
      __builtin_amdgcn_s_sleep(1);
    }
  }
  BARRIER();
  STAMP(7);
  unsigned nd2 = nd;
  if (wave == 0) nd2 = ld_agent(nonce_w + lane);

  {
    const auto rs_h = __builtin_amdgcn_make_buffer_rsrc((void*)ws, 0, H_BYTES, 0x00020000);
    const unsigned hb = ((unsigned)mb0 * KS2 + wave * 14) * 1024;
    const unsigned vo = lane * 16;
    const unsigned tail_off = wave == 3 ? 0x80000000u : 12 * 1024;
    u32x4 H[3][14];
    auto load_h1 = [&](int buf, int mb, int k) {
      const unsigned so = hb + (unsigned)mb * (KS2 * 1024);
      if (k < 12) H[buf][k] = __builtin_amdgcn_raw_buffer_load_b128(rs_h, vo + (k & 3) * 1024, so + (k >> 2) * 4096, 16);
      else H[buf][k] = __builtin_amdgcn_raw_buffer_load_b128(rs_h, vo + (k & 3) * 1024, so + tail_off, 16);
    };
#pragma unroll
    for (int k = 0; k < 14; ++k) load_h1(0, 0, k);
#pragma unroll
    for (int k = 0; k < 14; ++k) load_h1(1, 1, k);
    SB();
    const int m = lane & 15, g = lane >> 4;
#pragma unroll
    for (int mb = 0; mb < 6; ++mb) {
      if (mb < 4 || mb < nmb) {
        f32x4 acc[4];
#pragma unroll
        for (int nb = 0; nb < 4; ++nb) acc[nb] = f32x4{0.f, 0.f, 0.f, 0.f};
        const int mbn = mb + 2 < nmb ? mb + 2 : nmb - 1;
#pragma unroll
        for (int k = 0; k < 14; ++k) {
#pragma unroll
          for (int nb = 0; nb < 4; ++nb) {
            acc[nb] = __builtin_amdgcn_mfma_f32_16x16x32_bf16(W[k][nb], __builtin_bit_cast(bf16x8, H[mb % 3][k]), acc[nb], 0, 0, 0);
            if (nb == 0 && mb + 2 < 6) { SB(); load_h1((mb + 2) % 3, mbn, k); SB(); }
          }
        }
#pragma unroll
        for (int nb = 0; nb < 4; ++nb)
          *(LAS f32x4*)(smem + mb * 16384 + wave * 4096 + nb * 1024 + g * 256 + ((m ^ (4 * nb + (g & 2))) << 4)) = acc[nb];
        SB();
      }
    }
    WAIT_LGKM0();
    BARRIER();
    STAMP(8);
    const auto rs_o = __builtin_amdgcn_make_buffer_rsrc((void*)out, 0, OUT_BYTES, 0x00020000);
    const int c = lane & 7, nb = c >> 1;
#pragma unroll 1
    for (int u = wave; u < 2 * nmb; u += 4) {
      const int mb = u >> 1, mr = (u & 1) * 8 + (lane >> 3);
      LAS char* base = smem + mb * 16384 + nb * 1024 + (c & 1) * 512 + ((mr ^ (2 * c)) << 4);
      f32x4 lo = *(LAS f32x4*)base, hi4 = *(LAS f32x4*)(base + 256);
#pragma unroll
      for (int wv = 1; wv < 4; ++wv) { lo += *(LAS f32x4*)(base + wv * 4096); hi4 += *(LAS f32x4*)(base + wv * 4096 + 256); }
      u32x4 v;
      v[0] = pack_bf16(lo[0], lo[1]); v[1] = pack_bf16(lo[2], lo[3]); v[2] = pack_bf16(hi4[0], hi4[1]); v[3] = pack_bf16(hi4[2], hi4[3]);
      __builtin_amdgcn_raw_buffer_store_b128(v, rs_o, (unsigned)((mb0 + mb) * 16 + mr) * (kD * 2) + c * 16, lc2 * 128, 0);
    }
  }
  if (wave == 0 && nd2 != nd) st_agent(inbox + lane * 64 + member, nd2);
  STAMP(9);
}

extern "C" void kernel_launch(void* const* d_in, const int* in_sizes, int n_in, void* d_out, int out_size, void* d_ws,
                              size_t ws_size, hipStream_t stream) {
  (void)in_sizes; (void)n_in; (void)out_size; (void)ws_size;
  hipFuncSetAttribute((const void*)ffn_relu_kernel, hipFuncAttributeMaxDynamicSharedMemorySize, LDS_BYTES);
  ffn_relu_kernel<<<256, 256, LDS_BYTES, stream>>>((const char*)d_in[0], (const char*)d_in[1], (const char*)d_in[2],
                                                   (char*)d_out, (char*)d_ws);
}
```

```cpp
#include <hip/hip_runtime.h>
#include <stdint.h>

typedef short bf16x8 __attribute__((ext_vector_type(8)));
typedef short s16x4 __attribute__((ext_vector_type(4)));
typedef float f32x4 __attribute__((ext_vector_type(4)));
typedef float f32x2 __attribute__((ext_vector_type(2)));
typedef float f32x16 __attribute__((ext_vector_type(16)));
typedef unsigned u32x4 __attribute__((ext_vector_type(4)));
typedef __bf16 bf16x2_t __attribute__((ext_vector_type(2)));
#define LAS __attribute__((address_space(3)))
#define GAS __attribute__((address_space(1)))
#define DI __device__ __forceinline__

#define HDIST 12
#define MAP2 0
#define ROTW 10
#define W2ROT 0
#define WARM0 1
#define PF2 2
#define PULL 1
#define FINE2 0
#define STAMPS 0

namespace {
constexpr int kM = 2048, kD = 640, kF = 1728;
constexpr unsigned X_BYTES = kM * kD * 2, W1_BYTES = kD * kF * 2, W2_BYTES = kF * kD * 2, OUT_BYTES = kM * kD * 2;
constexpr unsigned XROW = kD * 2, W1ROW = kF * 2, W2ROW = kD * 2;
constexpr int KS2 = kF / 32;
constexpr unsigned H_BYTES = (kM / 16) * KS2 * 1024;
constexpr unsigned SLOT = 32768, LDS_BYTES = 5 * SLOT;
constexpr size_t WS_NONCE = 8u << 20, WS_INBOX = WS_NONCE + 4096, WS_STAMP = WS_INBOX + 65536;

DI u32x4 mk_rsrc(const void* p, unsigned bytes) {
  uint64_t a = (uint64_t)p;
  u32x4 r;
  r[0] = (unsigned)a; r[1] = (unsigned)(a >> 32) & 0xffffu; r[2] = bytes; r[3] = 0x00020000u;
  return r;
}
template <int OFF>
DI void dma2(unsigned m0v, unsigned va, unsigned vb, u32x4 rs, unsigned soff) {
  asm volatile("s_mov_b32 m0, %0\n\ts_nop 0\n\t"
               "buffer_load_dwordx4 %1, %3, %4 offen offset:%5 lds\n\t"
               "buffer_load_dwordx4 %2, %3, %4 offen offset:%6 lds"
               :: "s"(m0v), "v"(va), "v"(vb), "s"(rs), "s"(soff), "i"(OFF), "i"(OFF + 1024) : "memory");
}
template <int OFF>
DI void dma_m(unsigned m0v, unsigned va, u32x4 rs, unsigned soff) {
  asm volatile("s_mov_b32 m0, %0\n\ts_nop 0\n\t"
               "buffer_load_dwordx4 %1, %2, %3 offen offset:%4 lds"
               :: "s"(m0v), "v"(va), "s"(rs), "s"(soff), "i"(OFF) : "memory");
}
template <int OFF>
DI void dma_n(unsigned va, u32x4 rs, unsigned soff) {
  asm volatile("buffer_load_dwordx4 %0, %1, %2 offen offset:%3 lds" :: "v"(va), "s"(rs), "s"(soff), "i"(OFF) : "memory");
}
#define SB() __builtin_amdgcn_sched_barrier(0)
#define WAIT_VM(n) asm volatile("s_waitcnt vmcnt(" #n ")" ::: "memory")
#define WAIT_LGKM0() asm volatile("s_waitcnt lgkmcnt(0)" ::: "memory")
#define BARRIER() do { asm volatile("" ::: "memory"); __builtin_amdgcn_s_barrier(); asm volatile("" ::: "memory"); } while (0)

DI bf16x8 tr_read2(LAS char* p, int second_off) {
  s16x4 a = __builtin_amdgcn_ds_read_tr16_b64_v4i16((LAS s16x4*)p);
  s16x4 b = __builtin_amdgcn_ds_read_tr16_b64_v4i16((LAS s16x4*)(p + second_off));
  return __builtin_shufflevector(a, b, 0, 1, 2, 3, 4, 5, 6, 7);
}
DI unsigned pack_bf16(float a, float b) {
  f32x2 v = {a, b};
  return __builtin_bit_cast(unsigned, __builtin_convertvector(v, bf16x2_t));
}
DI float relu_nan(float v) { return __builtin_elementwise_maximum(v, 0.0f); }
DI unsigned ld_agent(const unsigned* p) { return __hip_atomic_load((const GAS unsigned*)p, __ATOMIC_RELAXED, __HIP_MEMORY_SCOPE_AGENT); }
DI void st_agent(unsigned* p, unsigned v) { __hip_atomic_store((GAS unsigned*)p, v, __ATOMIC_RELAXED, __HIP_MEMORY_SCOPE_AGENT); }

}

#define STAMP(i) do {} while (0)
#define CSTAMP(i) do {} while (0)
#define CSTAMP2(i) do {} while (0)

__global__ __launch_bounds__(256, 1) void ffn_relu_kernel(const char* __restrict__ x, const char* __restrict__ w1,
                                                          const char* __restrict__ w2, char* __restrict__ out,
                                                          char* __restrict__ ws) {
  extern __shared__ __attribute__((aligned(16))) char smem_g[];
  LAS char* smem = (LAS char*)smem_g;
  const unsigned lds0 = (unsigned)(size_t)smem;
  const int lane = threadIdx.x & 63;
  const int wave = __builtin_amdgcn_readfirstlane(threadIdx.x >> 6);
  const int b = blockIdx.x, xg = b & 7, idx = b >> 3;
  const int grp = xg >> 1, par = xg & 1;
  const int member = par * 32 + idx;
  STAMP(0);

  unsigned* nonce_w = (unsigned*)(ws + WS_NONCE) + grp * 64;
  unsigned* inbox = (unsigned*)(ws + WS_INBOX) + (size_t)grp * 64 * 64;
#define BOX_RD (inbox + lane * 64 + member)
#define BOX_WR (inbox + member * 64 + lane)
  unsigned in0;
  asm volatile("global_load_dword %0, %1, off sc1" : "=v"(in0) : "v"(BOX_RD) : "memory");

  int strip, pos, nmb, mb0;
  if (idx < 14) { strip = idx >= 7; pos = idx - 7 * strip; nmb = pos < 4 ? 5 : 4; mb0 = pos < 4 ? 5 * pos : 4 + 4 * pos; }
  else { int j = idx - 14; strip = 2 + j / 6; pos = j % 6; nmb = pos < 4 ? 5 : 6; mb0 = pos < 4 ? 5 * pos : 6 * pos - 4; }
  mb0 += grp * 32;
  int lc2 = par * 5 + strip;

  const u32x4 rs_x = mk_rsrc(x, X_BYTES), rs_w1 = mk_rsrc(w1, W1_BYTES), rs_w2 = mk_rsrc(w2, W2_BYTES);
  unsigned v2[4];
#pragma unroll
  for (int i = 0; i < 4; ++i)
    v2[i] = (lane >> 3) * W2ROW + (((lane & 7) ^ ((((lane >> 4) & 1) | (((i >> 1) & 1) << 1)) << 1)) << 4) + i * (8 * W2ROW - 1024);
  const unsigned s2_base = (unsigned)(wave * 448) * W2ROW + lc2 * 128;

#define W2SLOT(j) ((j) == 0 || (j) == 5 ? 4 * SLOT : (j) == 1 || (j) == 6 ? 2 * SLOT : (j) == 2 ? 3 * SLOT : (j) == 3 ? 0u : SLOT)
#define PICK_NONCE() do { asm volatile("" : "+v"(in0)); \
    nonce = __builtin_amdgcn_readfirstlane(in0) + 1u; \
    while (__builtin_amdgcn_ballot_w64(in0 == nonce) != 0) ++nonce; \
    if (threadIdx.x == 0) st_agent(nonce_w + member, nonce); } while (0)
#define LOAD_ND() asm volatile("global_load_dword %0, %1, off sc1" : "=v"(nd) : "v"(nonce_w + lane) : "memory")
  const unsigned m0w2 = lds0 + wave * 8192;
  unsigned s2o[7], hko[7];
#pragma unroll
  for (int j = 0; j < 7; ++j) {
    int jr = j + (W2ROT ? pos : 0); if (jr >= 7) jr -= 7;
    s2o[j] = s2_base + (unsigned)jr * (64 * W2ROW);
    hko[j] = (wave == 3 && jr == 6) ? 0x80000000u : (unsigned)jr * 2048;
  }
  const bool has_p1 = (idx & 7) != 7;
  unsigned nonce, nd;
  if (has_p1) {
    const int pm = grp * 4 + (idx >> 3), pn = par * 7 + (idx & 7);
    const int col0 = pn == 13 ? kF - 128 : pn * 128;
    const int wm = wave >> 1, wf = wave & 1;
    int kr = (pn + 3 * pm) % ROTW, kb = 0, kn = 0;
    auto next_k = [&]() {
      const int k = kb + kr;
      kr = kr == ROTW - 1 ? 0 : kr + 1;
      if (ROTW < 10) { ++kn; if (kn == ROTW) { kn = 0; kb += ROTW; } }
      return k;
    };
    unsigned vx[4], vw[4];
#pragma unroll
    for (int i = 0; i < 4; ++i) {
      vx[i] = (lane >> 3) * XROW + (((lane & 7) ^ ((4 * (i & 1) + (lane >> 4)) & 7)) << 4) + i * (8 * XROW - 1024);
      vw[i] = (lane >> 4) * W1ROW + (((lane & 15) ^ ((lane >> 4) << 2)) << 4) + i * (4 * W1ROW - 1024);
    }
    const unsigned sx_base = (unsigned)(pm * 128 + wave * 32) * XROW, sw_base = (unsigned)(wave * 16) * W1ROW + col0 * 2;
    const unsigned m0base = lds0 + wave * 4096;
    const int hi = lane >> 5, r31 = lane & 31;
    LAS char* xr[4][2]; LAS char* wr[2][2];
#define SETUP_READ() do { \
    const unsigned xrow = wm * 64 + r31; \
    const unsigned xa0 = xrow * 128 + ((hi ^ ((xrow >> 1) & 7)) << 4); \
    const int fh = (lane >> 4) & 1, q = (lane & 15) >> 2, p = lane & 3; \
    const unsigned wa0 = 16384 + (8 * hi + q) * 256 + (((8 * wf + 2 * fh + (p >> 1)) ^ (q << 2)) << 4) + 8 * (p & 1); \
    _Pragma("unroll") for (int h = 0; h < 2; ++h) { \
      _Pragma("unroll") for (int k = 0; k < 4; ++k) { xr[k][h] = smem + (xa0 ^ (k << 5)) + h * 65536; asm volatile("" : "+v"(xr[k][h])); } \
      wr[0][h] = smem + wa0 + h * 65536; wr[1][h] = smem + (wa0 ^ 64) + h * 65536; asm volatile("" : "+v"(wr[0][h]), "+v"(wr[1][h])); \
    } } while (0)
    f32x16 acc[2][2];
    bf16x8 A[8][2], B[8][2];
#define MF0(i, j, a, b) asm volatile("v_mfma_f32_32x32x16_bf16 %0, %1, %2, 0" : "=&v"(acc[i][j]) : "v"(a), "v"(b))
#define MF1(i, j, a, b) asm volatile("v_mfma_f32_32x32x16_bf16 %0, %1, %2, %0" : "+v"(acc[i][j]) : "v"(a), "v"(b))
    const auto rs_h = __builtin_amdgcn_make_buffer_rsrc((void*)ws, 0, H_BYTES, 0x00020000);
    const unsigned dead = (pn == 13 && wf == 0) ? 0x80000000u : 0u;
    const unsigned hs_base = ((unsigned)(pm * 8 + wm * 4) * KS2 + (col0 >> 5) + 2 * wf) * 1024 + dead;
    const unsigned hs_vo = (r31 >> 4) * (KS2 * 1024) + hi * 512 + (lane & 15) * 16;
    u32x4 ev[2];
#define EPI_WORD(fb, mb, s, e) ev[s][e] = pack_bf16(relu_nan(acc[fb][mb][8 * (s) + 2 * (e)]), relu_nan(acc[fb][mb][8 * (s) + 2 * (e) + 1]))
#define EPI_STORE(fb, mb, s) __builtin_amdgcn_raw_buffer_store_b128(ev[s], rs_h, hs_vo + (s) * 256, hs_base + ((mb) * 2 * KS2 + (fb)) * 1024, 16)
    STAMP(1);
    const int k0 = next_k(); const unsigned sx0 = sx_base + k0 * 128, sw0 = sw_base + (unsigned)k0 * (64 * W1ROW); dma_m<0>(m0base + 0, vx[0], rs_x, sx0);
    dma_n<1024>(vx[1], rs_x, sx0);
    dma_n<2048>(vx[2], rs_x, sx0);
    dma_n<3072>(vx[3], rs_x, sx0);
    dma_m<0>(m0base + 16384, vw[0], rs_w1, sw0);
    dma_n<1024>(vw[1], rs_w1, sw0);
    dma_n<2048>(vw[2], rs_w1, sw0);
    dma_n<3072>(vw[3], rs_w1, sw0);
    const int k1 = next_k(); const unsigned sx1 = sx_base + k1 * 128, sw1 = sw_base + (unsigned)k1 * (64 * W1ROW); dma_m<0>(m0base + 32768, vx[0], rs_x, sx1);
    dma_n<1024>(vx[1], rs_x, sx1);
    dma_n<2048>(vx[2], rs_x, sx1);
    dma_n<3072>(vx[3], rs_x, sx1);
    dma_m<0>(m0base + 49152, vw[0], rs_w1, sw1);
    dma_n<1024>(vw[1], rs_w1, sw1);
    dma_n<2048>(vw[2], rs_w1, sw1);
    dma_n<3072>(vw[3], rs_w1, sw1);
    const int k2 = next_k(); const unsigned sx2 = sx_base + k2 * 128, sw2 = sw_base + (unsigned)k2 * (64 * W1ROW); dma_m<0>(m0base + 65536, vx[0], rs_x, sx2);
    dma_n<1024>(vx[1], rs_x, sx2);
    dma_n<2048>(vx[2], rs_x, sx2);
    dma_n<3072>(vx[3], rs_x, sx2);
    dma_m<0>(m0base + 81920, vw[0], rs_w1, sw2);
    dma_n<1024>(vw[1], rs_w1, sw2);
    dma_n<2048>(vw[2], rs_w1, sw2);
    dma_n<3072>(vw[3], rs_w1, sw2);
    STAMP(2);
    WAIT_VM(24);
    PICK_NONCE();
    SETUP_READ();
    WAIT_VM(16); BARRIER();
    STAMP(3); CSTAMP(0);
    B[0][0] = *(LAS bf16x8*)(xr[0][0] + 0); B[0][1] = *(LAS bf16x8*)(xr[0][0] + 4096); A[0][0] = tr_read2(wr[0][0] + 0, 1024); A[0][1] = tr_read2(wr[1][0] + 0, 1024); B[1][0] = *(LAS bf16x8*)(xr[1][0] + 0); B[1][1] = *(LAS bf16x8*)(xr[1][0] + 4096); A[1][0] = tr_read2(wr[0][0] + 4096, 1024); A[1][1] = tr_read2(wr[1][0] + 4096, 1024); SB();
    MF0(0, 0, A[0][0], B[0][0]); SB();
    B[2][0] = *(LAS bf16x8*)(xr[2][0] + 0); B[2][1] = *(LAS bf16x8*)(xr[2][0] + 4096); const int k3 = next_k(); const unsigned sx3 = sx_base + k3 * 128, sw3 = sw_base + (unsigned)k3 * (64 * W1ROW); dma_m<0>(m0base + 98304, vx[0], rs_x, sx3); SB();
    MF0(0, 1, A[0][0], B[0][1]); SB();
    A[2][0] = tr_read2(wr[0][0] + 8192, 1024); A[2][1] = tr_read2(wr[1][0] + 8192, 1024); SB();
    MF0(1, 1, A[0][1], B[0][1]); SB();
    dma_n<1024>(vx[1], rs_x, sx3); SB();
    MF0(1, 0, A[0][1], B[0][0]); SB();
    dma_n<2048>(vx[2], rs_x, sx3); SB();
    MF1(0, 0, A[1][0], B[1][0]); SB();
    B[3][0] = *(LAS bf16x8*)(xr[3][0] + 0); B[3][1] = *(LAS bf16x8*)(xr[3][0] + 4096); dma_n<3072>(vx[3], rs_x, sx3); SB();
    MF1(0, 1, A[1][0], B[1][1]); SB();
    A[3][0] = tr_read2(wr[0][0] + 12288, 1024); A[3][1] = tr_read2(wr[1][0] + 12288, 1024); SB();
    MF1(1, 1, A[1][1], B[1][1]); SB();
    dma_m<0>(m0base + 114688, vw[0], rs_w1, sw3); SB();
    MF1(1, 0, A[1][1], B[1][0]); SB();
    WAIT_LGKM0(); WAIT_VM(13); BARRIER(); CSTAMP(1); SB();
    MF1(0, 0, A[2][0], B[2][0]); SB();
    B[4][0] = *(LAS bf16x8*)(xr[0][0] + 32768); B[4][1] = *(LAS bf16x8*)(xr[0][0] + 36864); dma_n<1024>(vw[1], rs_w1, sw3); SB();
    MF1(0, 1, A[2][0], B[2][1]); SB();
    A[4][0] = tr_read2(wr[0][0] + 32768, 1024); A[4][1] = tr_read2(wr[1][0] + 32768, 1024); SB();
    MF1(1, 1, A[2][1], B[2][1]); SB();
    dma_n<2048>(vw[2], rs_w1, sw3); SB();
    MF1(1, 0, A[2][1], B[2][0]); SB();
    dma_n<3072>(vw[3], rs_w1, sw3); SB();
    MF1(0, 0, A[3][0], B[3][0]); SB();
    B[5][0] = *(LAS bf16x8*)(xr[1][0] + 32768); B[5][1] = *(LAS bf16x8*)(xr[1][0] + 36864); const int k4 = next_k(); const unsigned sx4 = sx_base + k4 * 128, sw4 = sw_base + (unsigned)k4 * (64 * W1ROW); dma_m<0>(m0base + 0, vx[0], rs_x, sx4); SB();
    MF1(0, 1, A[3][0], B[3][1]); SB();
    A[5][0] = tr_read2(wr[0][0] + 36864, 1024); A[5][1] = tr_read2(wr[1][0] + 36864, 1024); SB();
    MF1(1, 1, A[3][1], B[3][1]); SB();
    dma_n<1024>(vx[1], rs_x, sx4); SB();
    MF1(1, 0, A[3][1], B[3][0]); SB();
    dma_n<2048>(vx[2], rs_x, sx4); SB();
    MF1(0, 0, A[4][0], B[4][0]); SB();
    B[6][0] = *(LAS bf16x8*)(xr[2][0] + 32768); B[6][1] = *(LAS bf16x8*)(xr[2][0] + 36864); dma_n<3072>(vx[3], rs_x, sx4); SB();
    MF1(0, 1, A[4][0], B[4][1]); SB();
    A[6][0] = tr_read2(wr[0][0] + 40960, 1024); A[6][1] = tr_read2(wr[1][0] + 40960, 1024); SB();
    MF1(1, 1, A[4][1], B[4][1]); SB();
    dma_m<0>(m0base + 16384, vw[0], rs_w1, sw4); SB();
    MF1(1, 0, A[4][1], B[4][0]); SB();
    MF1(0, 0, A[5][0], B[5][0]); SB();
    B[7][0] = *(LAS bf16x8*)(xr[3][0] + 32768); B[7][1] = *(LAS bf16x8*)(xr[3][0] + 36864); SB();
    MF1(0, 1, A[5][0], B[5][1]); SB();
    A[7][0] = tr_read2(wr[0][0] + 45056, 1024); A[7][1] = tr_read2(wr[1][0] + 45056, 1024); SB();
    MF1(1, 1, A[5][1], B[5][1]); SB();
    MF1(1, 0, A[5][1], B[5][0]); SB();
    WAIT_LGKM0(); WAIT_VM(13); BARRIER(); CSTAMP(2); SB();
    MF1(0, 0, A[6][0], B[6][0]); SB();
    B[0][0] = *(LAS bf16x8*)(xr[0][1] + 0); B[0][1] = *(LAS bf16x8*)(xr[0][1] + 4096); dma_n<1024>(vw[1], rs_w1, sw4); SB();
    MF1(0, 1, A[6][0], B[6][1]); SB();
    A[0][0] = tr_read2(wr[0][1] + 0, 1024); A[0][1] = tr_read2(wr[1][1] + 0, 1024); SB();
    MF1(1, 1, A[6][1], B[6][1]); SB();
    dma_n<2048>(vw[2], rs_w1, sw4); SB();
    MF1(1, 0, A[6][1], B[6][0]); SB();
    dma_n<3072>(vw[3], rs_w1, sw4); SB();
    MF1(0, 0, A[7][0], B[7][0]); SB();
    B[1][0] = *(LAS bf16x8*)(xr[1][1] + 0); B[1][1] = *(LAS bf16x8*)(xr[1][1] + 4096); const int k5 = next_k(); const unsigned sx5 = sx_base + k5 * 128, sw5 = sw_base + (unsigned)k5 * (64 * W1ROW); dma_m<0>(m0base + 32768, vx[0], rs_x, sx5); SB();
    MF1(0, 1, A[7][0], B[7][1]); SB();
    A[1][0] = tr_read2(wr[0][1] + 4096, 1024); A[1][1] = tr_read2(wr[1][1] + 4096, 1024); SB();
    MF1(1, 1, A[7][1], B[7][1]); SB();
    dma_n<1024>(vx[1], rs_x, sx5); SB();
    MF1(1, 0, A[7][1], B[7][0]); SB();
    dma_n<2048>(vx[2], rs_x, sx5); SB();
    MF1(0, 0, A[0][0], B[0][0]); SB();
    B[2][0] = *(LAS bf16x8*)(xr[2][1] + 0); B[2][1] = *(LAS bf16x8*)(xr[2][1] + 4096); dma_n<3072>(vx[3], rs_x, sx5); SB();
    MF1(0, 1, A[0][0], B[0][1]); SB();
    A[2][0] = tr_read2(wr[0][1] + 8192, 1024); A[2][1] = tr_read2(wr[1][1] + 8192, 1024); SB();
    MF1(1, 1, A[0][1], B[0][1]); SB();
    dma_m<0>(m0base + 49152, vw[0], rs_w1, sw5); SB();
    MF1(1, 0, A[0][1], B[0][0]); SB();
    MF1(0, 0, A[1][0], B[1][0]); SB();
    B[3][0] = *(LAS bf16x8*)(xr[3][1] + 0); B[3][1] = *(LAS bf16x8*)(xr[3][1] + 4096); SB();
    MF1(0, 1, A[1][0], B[1][1]); SB();
    A[3][0] = tr_read2(wr[0][1] + 12288, 1024); A[3][1] = tr_read2(wr[1][1] + 12288, 1024); SB();
    MF1(1, 1, A[1][1], B[1][1]); SB();
    MF1(1, 0, A[1][1], B[1][0]); SB();
    WAIT_LGKM0(); WAIT_VM(13); BARRIER(); CSTAMP(3); SB();
    MF1(0, 0, A[2][0], B[2][0]); SB();
    B[4][0] = *(LAS bf16x8*)(xr[0][1] + 32768); B[4][1] = *(LAS bf16x8*)(xr[0][1] + 36864); dma_n<1024>(vw[1], rs_w1, sw5); SB();
    MF1(0, 1, A[2][0], B[2][1]); SB();
    A[4][0] = tr_read2(wr[0][1] + 32768, 1024); A[4][1] = tr_read2(wr[1][1] + 32768, 1024); SB();
    MF1(1, 1, A[2][1], B[2][1]); SB();
    dma_n<2048>(vw[2], rs_w1, sw5); SB();
    MF1(1, 0, A[2][1], B[2][0]); SB();
    dma_n<3072>(vw[3], rs_w1, sw5); SB();
    MF1(0, 0, A[3][0], B[3][0]); SB();
    B[5][0] = *(LAS bf16x8*)(xr[1][1] + 32768); B[5][1] = *(LAS bf16x8*)(xr[1][1] + 36864); const int k6 = next_k(); const unsigned sx6 = sx_base + k6 * 128, sw6 = sw_base + (unsigned)k6 * (64 * W1ROW); dma_m<0>(m0base + 65536, vx[0], rs_x, sx6); SB();
    MF1(0, 1, A[3][0], B[3][1]); SB();
    A[5][0] = tr_read2(wr[0][1] + 36864, 1024); A[5][1] = tr_read2(wr[1][1] + 36864, 1024); SB();
    MF1(1, 1, A[3][1], B[3][1]); SB();
    dma_n<1024>(vx[1], rs_x, sx6); SB();
    MF1(1, 0, A[3][1], B[3][0]); SB();
    dma_n<2048>(vx[2], rs_x, sx6); SB();
    MF1(0, 0, A[4][0], B[4][0]); SB();
    B[6][0] = *(LAS bf16x8*)(xr[2][1] + 32768); B[6][1] = *(LAS bf16x8*)(xr[2][1] + 36864); dma_n<3072>(vx[3], rs_x, sx6); SB();
    MF1(0, 1, A[4][0], B[4][1]); SB();
    A[6][0] = tr_read2(wr[0][1] + 40960, 1024); A[6][1] = tr_read2(wr[1][1] + 40960, 1024); SB();
    MF1(1, 1, A[4][1], B[4][1]); SB();
    dma_m<0>(m0base + 81920, vw[0], rs_w1, sw6); SB();
    MF1(1, 0, A[4][1], B[4][0]); SB();
    MF1(0, 0, A[5][0], B[5][0]); SB();
    B[7][0] = *(LAS bf16x8*)(xr[3][1] + 32768); B[7][1] = *(LAS bf16x8*)(xr[3][1] + 36864); SB();
    MF1(0, 1, A[5][0], B[5][1]); SB();
    A[7][0] = tr_read2(wr[0][1] + 45056, 1024); A[7][1] = tr_read2(wr[1][1] + 45056, 1024); SB();
    MF1(1, 1, A[5][1], B[5][1]); SB();
    MF1(1, 0, A[5][1], B[5][0]); SB();
    WAIT_LGKM0(); WAIT_VM(13); BARRIER(); CSTAMP(4); SB();
    MF1(0, 0, A[6][0], B[6][0]); SB();
    B[0][0] = *(LAS bf16x8*)(xr[0][0] + 0); B[0][1] = *(LAS bf16x8*)(xr[0][0] + 4096); dma_n<1024>(vw[1], rs_w1, sw6); SB();
    MF1(0, 1, A[6][0], B[6][1]); SB();
    A[0][0] = tr_read2(wr[0][0] + 0, 1024); A[0][1] = tr_read2(wr[1][0] + 0, 1024); SB();
    MF1(1, 1, A[6][1], B[6][1]); SB();
    dma_n<2048>(vw[2], rs_w1, sw6); SB();
    MF1(1, 0, A[6][1], B[6][0]); SB();
    dma_n<3072>(vw[3], rs_w1, sw6); SB();
    MF1(0, 0, A[7][0], B[7][0]); SB();
    B[1][0] = *(LAS bf16x8*)(xr[1][0] + 0); B[1][1] = *(LAS bf16x8*)(xr[1][0] + 4096); const int k7 = next_k(); const unsigned sx7 = sx_base + k7 * 128, sw7 = sw_base + (unsigned)k7 * (64 * W1ROW); dma_m<0>(m0base + 98304, vx[0], rs_x, sx7); SB();
    MF1(0, 1, A[7][0], B[7][1]); SB();
    A[1][0] = tr_read2(wr[0][0] + 4096, 1024); A[1][1] = tr_read2(wr[1][0] + 4096, 1024); SB();
    MF1(1, 1, A[7][1], B[7][1]); SB();
    dma_n<1024>(vx[1], rs_x, sx7); SB();
    MF1(1, 0, A[7][1], B[7][0]); SB();
    dma_n<2048>(vx[2], rs_x, sx7); SB();
    MF1(0, 0, A[0][0], B[0][0]); SB();
    B[2][0] = *(LAS bf16x8*)(xr[2][0] + 0); B[2][1] = *(LAS bf16x8*)(xr[2][0] + 4096); dma_n<3072>(vx[3], rs_x, sx7); SB();
    MF1(0, 1, A[0][0], B[0][1]); SB();
    A[2][0] = tr_read2(wr[0][0] + 8192, 1024); A[2][1] = tr_read2(wr[1][0] + 8192, 1024); SB();
    MF1(1, 1, A[0][1], B[0][1]); SB();
    dma_m<0>(m0base + 114688, vw[0], rs_w1, sw7); SB();
    MF1(1, 0, A[0][1], B[0][0]); SB();
    MF1(0, 0, A[1][0], B[1][0]); SB();
    B[3][0] = *(LAS bf16x8*)(xr[3][0] + 0); B[3][1] = *(LAS bf16x8*)(xr[3][0] + 4096); SB();
    MF1(0, 1, A[1][0], B[1][1]); SB();
    A[3][0] = tr_read2(wr[0][0] + 12288, 1024); A[3][1] = tr_read2(wr[1][0] + 12288, 1024); SB();
    MF1(1, 1, A[1][1], B[1][1]); SB();
    MF1(1, 0, A[1][1], B[1][0]); SB();
    WAIT_LGKM0(); WAIT_VM(13); BARRIER(); CSTAMP(5); SB();
    MF1(0, 0, A[2][0], B[2][0]); SB();
    B[4][0] = *(LAS bf16x8*)(xr[0][0] + 32768); B[4][1] = *(LAS bf16x8*)(xr[0][0] + 36864); dma_n<1024>(vw[1], rs_w1, sw7); SB();
    MF1(0, 1, A[2][0], B[2][1]); SB();
    A[4][0] = tr_read2(wr[0][0] + 32768, 1024); A[4][1] = tr_read2(wr[1][0] + 32768, 1024); SB();
    MF1(1, 1, A[2][1], B[2][1]); SB();
    dma_n<2048>(vw[2], rs_w1, sw7); SB();
    MF1(1, 0, A[2][1], B[2][0]); SB();
    dma_n<3072>(vw[3], rs_w1, sw7); SB();
    MF1(0, 0, A[3][0], B[3][0]); SB();
    B[5][0] = *(LAS bf16x8*)(xr[1][0] + 32768); B[5][1] = *(LAS bf16x8*)(xr[1][0] + 36864); const int k8 = next_k(); const unsigned sx8 = sx_base + k8 * 128, sw8 = sw_base + (unsigned)k8 * (64 * W1ROW); dma_m<0>(m0base + 0, vx[0], rs_x, sx8); SB();
    MF1(0, 1, A[3][0], B[3][1]); SB();
    A[5][0] = tr_read2(wr[0][0] + 36864, 1024); A[5][1] = tr_read2(wr[1][0] + 36864, 1024); SB();
    MF1(1, 1, A[3][1], B[3][1]); SB();
    dma_n<1024>(vx[1], rs_x, sx8); SB();
    MF1(1, 0, A[3][1], B[3][0]); SB();
    dma_n<2048>(vx[2], rs_x, sx8); SB();
    MF1(0, 0, A[4][0], B[4][0]); SB();
    B[6][0] = *(LAS bf16x8*)(xr[2][0] + 32768); B[6][1] = *(LAS bf16x8*)(xr[2][0] + 36864); dma_n<3072>(vx[3], rs_x, sx8); SB();
    MF1(0, 1, A[4][0], B[4][1]); SB();
    A[6][0] = tr_read2(wr[0][0] + 40960, 1024); A[6][1] = tr_read2(wr[1][0] + 40960, 1024); SB();
    MF1(1, 1, A[4][1], B[4][1]); SB();
    dma_m<0>(m0base + 16384, vw[0], rs_w1, sw8); SB();
    MF1(1, 0, A[4][1], B[4][0]); SB();
    MF1(0, 0, A[5][0], B[5][0]); SB();
    B[7][0] = *(LAS bf16x8*)(xr[3][0] + 32768); B[7][1] = *(LAS bf16x8*)(xr[3][0] + 36864); SB();
    MF1(0, 1, A[5][0], B[5][1]); SB();
    A[7][0] = tr_read2(wr[0][0] + 45056, 1024); A[7][1] = tr_read2(wr[1][0] + 45056, 1024); SB();
    MF1(1, 1, A[5][1], B[5][1]); SB();
    MF1(1, 0, A[5][1], B[5][0]); SB();
    WAIT_LGKM0(); WAIT_VM(13); BARRIER(); CSTAMP(6); SB();
    MF1(0, 0, A[6][0], B[6][0]); SB();
    B[0][0] = *(LAS bf16x8*)(xr[0][1] + 0); B[0][1] = *(LAS bf16x8*)(xr[0][1] + 4096); dma_n<1024>(vw[1], rs_w1, sw8); SB();
    MF1(0, 1, A[6][0], B[6][1]); SB();
    A[0][0] = tr_read2(wr[0][1] + 0, 1024); A[0][1] = tr_read2(wr[1][1] + 0, 1024); SB();
    MF1(1, 1, A[6][1], B[6][1]); SB();
    dma_n<2048>(vw[2], rs_w1, sw8); SB();
    MF1(1, 0, A[6][1], B[6][0]); SB();
    dma_n<3072>(vw[3], rs_w1, sw8); SB();
    MF1(0, 0, A[7][0], B[7][0]); SB();
    B[1][0] = *(LAS bf16x8*)(xr[1][1] + 0); B[1][1] = *(LAS bf16x8*)(xr[1][1] + 4096); const int k9 = next_k(); const unsigned sx9 = sx_base + k9 * 128, sw9 = sw_base + (unsigned)k9 * (64 * W1ROW); dma_m<0>(m0base + 32768, vx[0], rs_x, sx9); SB();
    MF1(0, 1, A[7][0], B[7][1]); SB();
    A[1][0] = tr_read2(wr[0][1] + 4096, 1024); A[1][1] = tr_read2(wr[1][1] + 4096, 1024); SB();
    MF1(1, 1, A[7][1], B[7][1]); SB();
    dma_n<1024>(vx[1], rs_x, sx9); SB();
    MF1(1, 0, A[7][1], B[7][0]); SB();
    dma_n<2048>(vx[2], rs_x, sx9); SB();
    MF1(0, 0, A[0][0], B[0][0]); SB();
    B[2][0] = *(LAS bf16x8*)(xr[2][1] + 0); B[2][1] = *(LAS bf16x8*)(xr[2][1] + 4096); dma_n<3072>(vx[3], rs_x, sx9); SB();
    MF1(0, 1, A[0][0], B[0][1]); SB();
    A[2][0] = tr_read2(wr[0][1] + 8192, 1024); A[2][1] = tr_read2(wr[1][1] + 8192, 1024); SB();
    MF1(1, 1, A[0][1], B[0][1]); SB();
    dma_m<0>(m0base + 49152, vw[0], rs_w1, sw9); SB();
    MF1(1, 0, A[0][1], B[0][0]); SB();
    MF1(0, 0, A[1][0], B[1][0]); SB();
    B[3][0] = *(LAS bf16x8*)(xr[3][1] + 0); B[3][1] = *(LAS bf16x8*)(xr[3][1] + 4096); SB();
    MF1(0, 1, A[1][0], B[1][1]); SB();
    A[3][0] = tr_read2(wr[0][1] + 12288, 1024); A[3][1] = tr_read2(wr[1][1] + 12288, 1024); SB();
    MF1(1, 1, A[1][1], B[1][1]); SB();
    MF1(1, 0, A[1][1], B[1][0]); SB();
    WAIT_LGKM0(); WAIT_VM(13); BARRIER(); CSTAMP(7); SB();
    MF1(0, 0, A[2][0], B[2][0]); SB();
    B[4][0] = *(LAS bf16x8*)(xr[0][1] + 32768); B[4][1] = *(LAS bf16x8*)(xr[0][1] + 36864); dma_n<1024>(vw[1], rs_w1, sw9); SB();
    MF1(0, 1, A[2][0], B[2][1]); SB();
    A[4][0] = tr_read2(wr[0][1] + 32768, 1024); A[4][1] = tr_read2(wr[1][1] + 32768, 1024); SB();
    MF1(1, 1, A[2][1], B[2][1]); SB();
    dma_n<2048>(vw[2], rs_w1, sw9); SB();
    MF1(1, 0, A[2][1], B[2][0]); SB();
    dma_n<3072>(vw[3], rs_w1, sw9); SB();
    MF1(0, 0, A[3][0], B[3][0]); SB();
    B[5][0] = *(LAS bf16x8*)(xr[1][1] + 32768); B[5][1] = *(LAS bf16x8*)(xr[1][1] + 36864); dma_m<0>(m0w2 + 131072, v2[0], rs_w2, s2o[0] + 0 * W2ROW); SB();
    MF1(0, 1, A[3][0], B[3][1]); SB();
    A[5][0] = tr_read2(wr[0][1] + 36864, 1024); A[5][1] = tr_read2(wr[1][1] + 36864, 1024); SB();
    MF1(1, 1, A[3][1], B[3][1]); SB();
    dma_n<1024>(v2[1], rs_w2, s2o[0] + 0 * W2ROW); SB();
    MF1(1, 0, A[3][1], B[3][0]); SB();
    dma_n<2048>(v2[2], rs_w2, s2o[0] + 0 * W2ROW); SB();
    MF1(0, 0, A[4][0], B[4][0]); SB();
    B[6][0] = *(LAS bf16x8*)(xr[2][1] + 32768); B[6][1] = *(LAS bf16x8*)(xr[2][1] + 36864); dma_n<3072>(v2[3], rs_w2, s2o[0] + 0 * W2ROW); SB();
    MF1(0, 1, A[4][0], B[4][1]); SB();
    A[6][0] = tr_read2(wr[0][1] + 40960, 1024); A[6][1] = tr_read2(wr[1][1] + 40960, 1024); SB();
    MF1(1, 1, A[4][1], B[4][1]); SB();
    dma_m<0>(m0w2 + 135168, v2[0], rs_w2, s2o[0] + 32 * W2ROW); SB();
    MF1(1, 0, A[4][1], B[4][0]); SB();
    dma_n<1024>(v2[1], rs_w2, s2o[0] + 32 * W2ROW); SB();
    MF1(0, 0, A[5][0], B[5][0]); SB();
    B[7][0] = *(LAS bf16x8*)(xr[3][1] + 32768); B[7][1] = *(LAS bf16x8*)(xr[3][1] + 36864); dma_n<2048>(v2[2], rs_w2, s2o[0] + 32 * W2ROW); SB();
    MF1(0, 1, A[5][0], B[5][1]); SB();
    A[7][0] = tr_read2(wr[0][1] + 45056, 1024); A[7][1] = tr_read2(wr[1][1] + 45056, 1024); SB();
    MF1(1, 1, A[5][1], B[5][1]); SB();
    dma_n<3072>(v2[3], rs_w2, s2o[0] + 32 * W2ROW); SB();
    MF1(1, 0, A[5][1], B[5][0]); SB();
    WAIT_LGKM0(); WAIT_VM(16); BARRIER(); CSTAMP(8); SB();
    MF1(0, 0, A[6][0], B[6][0]); SB();
    B[0][0] = *(LAS bf16x8*)(xr[0][0] + 0); B[1][0] = *(LAS bf16x8*)(xr[1][0] + 0); dma_m<0>(m0w2 + 65536, v2[0], rs_w2, s2o[1] + 0 * W2ROW); SB();
    MF1(0, 1, A[6][0], B[6][1]); SB();
    A[0][0] = tr_read2(wr[0][0] + 0, 1024); A[1][0] = tr_read2(wr[0][0] + 4096, 1024); SB();
    MF1(1, 1, A[6][1], B[6][1]); SB();
    dma_n<1024>(v2[1], rs_w2, s2o[1] + 0 * W2ROW); SB();
    MF1(1, 0, A[6][1], B[6][0]); SB();
    dma_n<2048>(v2[2], rs_w2, s2o[1] + 0 * W2ROW); SB();
    MF1(0, 0, A[7][0], B[7][0]); SB();
    B[2][0] = *(LAS bf16x8*)(xr[2][0] + 0); B[3][0] = *(LAS bf16x8*)(xr[3][0] + 0); dma_n<3072>(v2[3], rs_w2, s2o[1] + 0 * W2ROW); SB();
    MF1(0, 1, A[7][0], B[7][1]); SB();
    A[2][0] = tr_read2(wr[0][0] + 8192, 1024); A[3][0] = tr_read2(wr[0][0] + 12288, 1024); SB();
    MF1(1, 1, A[7][1], B[7][1]); SB();
    dma_m<0>(m0w2 + 69632, v2[0], rs_w2, s2o[1] + 32 * W2ROW); SB();
    MF1(1, 0, A[7][1], B[7][0]); SB();
    dma_n<1024>(v2[1], rs_w2, s2o[1] + 32 * W2ROW); SB();
    MF1(0, 0, A[0][0], B[0][0]); SB();
    B[0][1] = *(LAS bf16x8*)(xr[0][0] + 4096); B[1][1] = *(LAS bf16x8*)(xr[1][0] + 4096); dma_n<2048>(v2[2], rs_w2, s2o[1] + 32 * W2ROW); SB();
    MF1(0, 0, A[1][0], B[1][0]); SB();
    B[2][1] = *(LAS bf16x8*)(xr[2][0] + 4096); B[3][1] = *(LAS bf16x8*)(xr[3][0] + 4096); dma_n<3072>(v2[3], rs_w2, s2o[1] + 32 * W2ROW); SB();
    MF1(0, 0, A[2][0], B[2][0]); SB();
    WAIT_VM(16); BARRIER(); CSTAMP(9); SB();
    MF1(0, 0, A[3][0], B[3][0]); SB();
    B[4][0] = *(LAS bf16x8*)(xr[0][0] + 32768); A[4][0] = tr_read2(wr[0][0] + 32768, 1024); dma_m<0>(m0w2 + 98304, v2[0], rs_w2, s2o[2] + 0 * W2ROW); SB();
    MF1(0, 1, A[0][0], B[0][1]); SB();
    B[5][0] = *(LAS bf16x8*)(xr[1][0] + 32768); A[5][0] = tr_read2(wr[0][0] + 36864, 1024); dma_n<1024>(v2[1], rs_w2, s2o[2] + 0 * W2ROW); SB();
    MF1(0, 1, A[1][0], B[1][1]); SB();
    B[6][0] = *(LAS bf16x8*)(xr[2][0] + 32768); A[6][0] = tr_read2(wr[0][0] + 40960, 1024); dma_n<2048>(v2[2], rs_w2, s2o[2] + 0 * W2ROW); SB();
    MF1(0, 1, A[2][0], B[2][1]); SB();
    B[7][0] = *(LAS bf16x8*)(xr[3][0] + 32768); A[7][0] = tr_read2(wr[0][0] + 45056, 1024); dma_n<3072>(v2[3], rs_w2, s2o[2] + 0 * W2ROW); SB();
    MF1(0, 1, A[3][0], B[3][1]); SB();
    B[4][1] = *(LAS bf16x8*)(xr[0][0] + 36864); B[5][1] = *(LAS bf16x8*)(xr[1][0] + 36864); SB();
    MF1(0, 0, A[4][0], B[4][0]); SB();
    B[6][1] = *(LAS bf16x8*)(xr[2][0] + 36864); B[7][1] = *(LAS bf16x8*)(xr[3][0] + 36864); SB();
    MF1(0, 0, A[5][0], B[5][0]); SB();
    A[0][1] = tr_read2(wr[1][0] + 0, 1024); LOAD_ND(); SB();
    MF1(0, 0, A[6][0], B[6][0]); SB();
    A[1][1] = tr_read2(wr[1][0] + 4096, 1024); SB();
    MF1(0, 0, A[7][0], B[7][0]); SB();
    A[2][1] = tr_read2(wr[1][0] + 8192, 1024); SB();
    MF1(0, 1, A[4][0], B[4][1]); SB();
    A[3][1] = tr_read2(wr[1][0] + 12288, 1024); SB();
    MF1(0, 1, A[5][0], B[5][1]); SB();
    A[4][1] = tr_read2(wr[1][0] + 32768, 1024); EPI_WORD(0, 0, 0, 0); SB();
    MF1(0, 1, A[6][0], B[6][1]); SB();
    A[5][1] = tr_read2(wr[1][0] + 36864, 1024); EPI_WORD(0, 0, 0, 1); SB();
    MF1(0, 1, A[7][0], B[7][1]); SB();
    A[6][1] = tr_read2(wr[1][0] + 40960, 1024); EPI_WORD(0, 0, 0, 2); SB();
    MF1(1, 1, A[0][1], B[0][1]); SB();
    A[7][1] = tr_read2(wr[1][0] + 45056, 1024); EPI_WORD(0, 0, 0, 3); SB();
    MF1(1, 1, A[1][1], B[1][1]); SB();
    SB(); EPI_STORE(0, 0, 0); SB(); EPI_WORD(0, 0, 1, 0); EPI_WORD(0, 0, 1, 1); SB();
    MF1(1, 1, A[2][1], B[2][1]); SB();
    EPI_WORD(0, 0, 1, 2); EPI_WORD(0, 0, 1, 3); SB();
    MF1(1, 1, A[3][1], B[3][1]); SB();
    SB(); EPI_STORE(0, 0, 1); SB(); EPI_WORD(0, 1, 0, 0); EPI_WORD(0, 1, 0, 1); SB();
    MF1(1, 1, A[4][1], B[4][1]); SB();
    EPI_WORD(0, 1, 0, 2); EPI_WORD(0, 1, 0, 3); SB();
    MF1(1, 1, A[5][1], B[5][1]); SB();
    SB(); EPI_STORE(0, 1, 0); SB(); EPI_WORD(0, 1, 1, 0); EPI_WORD(0, 1, 1, 1); SB();
    MF1(1, 1, A[6][1], B[6][1]); SB();
    WAIT_LGKM0(); BARRIER(); CSTAMP(10); SB();
    MF1(1, 1, A[7][1], B[7][1]); SB();
    EPI_WORD(0, 1, 1, 2); EPI_WORD(0, 1, 1, 3); SB();
    MF1(1, 0, A[0][1], B[0][0]); SB();
    SB(); EPI_STORE(0, 1, 1); SB(); dma_m<0>(m0w2 + 102400, v2[0], rs_w2, s2o[2] + 32 * W2ROW); SB();
    MF1(1, 0, A[1][1], B[1][0]); SB();
    EPI_WORD(1, 1, 0, 0); EPI_WORD(1, 1, 0, 1); SB();
    MF1(1, 0, A[2][1], B[2][0]); SB();
    EPI_WORD(1, 1, 0, 2); EPI_WORD(1, 1, 0, 3); SB();
    MF1(1, 0, A[3][1], B[3][0]); SB();
    SB(); EPI_STORE(1, 1, 0); SB(); EPI_WORD(1, 1, 1, 0); EPI_WORD(1, 1, 1, 1); SB();
    MF1(1, 0, A[4][1], B[4][0]); SB();
    EPI_WORD(1, 1, 1, 2); EPI_WORD(1, 1, 1, 3); SB();
    MF1(1, 0, A[5][1], B[5][0]); SB();
    SB(); EPI_STORE(1, 1, 1); SB(); dma_n<1024>(v2[1], rs_w2, s2o[2] + 32 * W2ROW); SB();
    MF1(1, 0, A[6][1], B[6][0]); SB();
    dma_n<2048>(v2[2], rs_w2, s2o[2] + 32 * W2ROW); SB();
    MF1(1, 0, A[7][1], B[7][0]); SB();
    dma_n<3072>(v2[3], rs_w2, s2o[2] + 32 * W2ROW); SB();
    STAMP(4); CSTAMP(11);
    asm volatile("s_nop 15" ::: "memory"); SB();
    EPI_WORD(1, 0, 0, 0); EPI_WORD(1, 0, 0, 1); EPI_WORD(1, 0, 0, 2); EPI_WORD(1, 0, 0, 3); SB(); EPI_STORE(1, 0, 0); SB();
    EPI_WORD(1, 0, 1, 0); EPI_WORD(1, 0, 1, 1); EPI_WORD(1, 0, 1, 2); EPI_WORD(1, 0, 1, 3); SB(); EPI_STORE(1, 0, 1); SB();
    STAMP(10);
  } else {
    WAIT_VM(0);
    PICK_NONCE();
    __builtin_amdgcn_s_sleep(32);
    if (PF2) {
      const auto rx = __builtin_amdgcn_make_buffer_rsrc((void*)x, 0, X_BYTES, 0x00020000);
      const auto r1 = __builtin_amdgcn_make_buffer_rsrc((void*)w1, 0, W1_BYTES, 0x00020000);
      const auto r2 = __builtin_amdgcn_make_buffer_rsrc((void*)w2, 0, W2_BYTES, 0x00020000);
      u32x4 t = {0, 0, 0, 0};
      const unsigned q4 = idx >> 3;
      if (PF2 & 1) {
        const unsigned xo = (unsigned)(grp * 512 + (par * 4 + q4) * 64 + wave * 16) * XROW + 640;
#pragma unroll
        for (int i = 0; i < 10; ++i) { const unsigned c = i * 64 + lane; t ^= __builtin_amdgcn_raw_buffer_load_b128(rx, (c / 40) * XROW + (c % 40) * 16, xo, 0); }
        const unsigned wo = (unsigned)(320 + (grp * 4 + q4) * 20 + wave * 5) * W1ROW + par * 1664;
#pragma unroll
        for (int i = 0; i < 9; ++i) { unsigned c = i * 64 + lane; c = c < 560 ? c : 559; t ^= __builtin_amdgcn_raw_buffer_load_b128(r1, (c / 112) * W1ROW + (c % 112) * 16, wo, 0); }
      }
      if (PF2 & 2) {
        const unsigned wo2 = (unsigned)((grp * 4 + q4) * 108 + wave * 27) * W2ROW + par * 640;
#pragma unroll
        for (int i = 0; i < 17; ++i) { unsigned c = i * 64 + lane; c = c < 1080 ? c : 1079; t ^= __builtin_amdgcn_raw_buffer_load_b128(r2, (c / 40) * W2ROW + (c % 40) * 16, wo2, 0); }
      }
      asm volatile("" :: "v"(t));
    }
    dma_m<0>(m0w2 + 131072, v2[0], rs_w2, s2o[0] + 0 * W2ROW);
    dma_n<1024>(v2[1], rs_w2, s2o[0] + 0 * W2ROW);
    dma_n<2048>(v2[2], rs_w2, s2o[0] + 0 * W2ROW);
    dma_n<3072>(v2[3], rs_w2, s2o[0] + 0 * W2ROW);
    dma_m<0>(m0w2 + 135168, v2[0], rs_w2, s2o[0] + 32 * W2ROW);
    dma_n<1024>(v2[1], rs_w2, s2o[0] + 32 * W2ROW);
    dma_n<2048>(v2[2], rs_w2, s2o[0] + 32 * W2ROW);
    dma_n<3072>(v2[3], rs_w2, s2o[0] + 32 * W2ROW);
    dma_m<0>(m0w2 + 65536, v2[0], rs_w2, s2o[1] + 0 * W2ROW);
    dma_n<1024>(v2[1], rs_w2, s2o[1] + 0 * W2ROW);
    dma_n<2048>(v2[2], rs_w2, s2o[1] + 0 * W2ROW);
    dma_n<3072>(v2[3], rs_w2, s2o[1] + 0 * W2ROW);
    dma_m<0>(m0w2 + 69632, v2[0], rs_w2, s2o[1] + 32 * W2ROW);
    dma_n<1024>(v2[1], rs_w2, s2o[1] + 32 * W2ROW);
    dma_n<2048>(v2[2], rs_w2, s2o[1] + 32 * W2ROW);
    dma_n<3072>(v2[3], rs_w2, s2o[1] + 32 * W2ROW);
    dma_m<0>(m0w2 + 98304, v2[0], rs_w2, s2o[2] + 0 * W2ROW);
    dma_n<1024>(v2[1], rs_w2, s2o[2] + 0 * W2ROW);
    dma_n<2048>(v2[2], rs_w2, s2o[2] + 0 * W2ROW);
    dma_n<3072>(v2[3], rs_w2, s2o[2] + 0 * W2ROW);
    dma_m<0>(m0w2 + 102400, v2[0], rs_w2, s2o[2] + 32 * W2ROW);
    dma_n<1024>(v2[1], rs_w2, s2o[2] + 32 * W2ROW);
    dma_n<2048>(v2[2], rs_w2, s2o[2] + 32 * W2ROW);
    dma_n<3072>(v2[3], rs_w2, s2o[2] + 32 * W2ROW);
    WAIT_VM(24);
    if (WARM0) {
      const auto rw = __builtin_amdgcn_make_buffer_rsrc((void*)w2, 0, W2_BYTES, 0x00020000);
      const unsigned r0 = (idx >> 3) * 432 + wave * 108;
      u32x4 t = {0, 0, 0, 0};
#pragma unroll
      for (int i = 0; i < 14; ++i) t ^= __builtin_amdgcn_raw_buffer_load_b128(rw, ((lane >> 3) + 8 * i) * W2ROW + (lane & 7) * 16, r0 * W2ROW + par * 640, 0);
      asm volatile("" :: "v"(t));
    }
    WAIT_VM(0);
    LOAD_ND();
    WAIT_VM(0);
  }

  bf16x8 W[14][4];
  {
    const int g = lane >> 4, q = (lane & 15) >> 2, p = lane & 3;
    const unsigned f = ((q >> 1) & 1) | ((g & 1) << 1);
    const unsigned ra = wave * 8192 + (16 * (g & 1) + 4 * (g >> 1) + q) * 128 + ((p >> 1) << 4) + 8 * (p & 1);
    LAS char* wb[4];
#pragma unroll
    for (int nb = 0; nb < 4; ++nb) wb[nb] = smem + ra + ((nb ^ f) << 5);
#define RW(j, k, nb) W[2 * (j) + (k)][nb] = tr_read2(wb[nb] + W2SLOT(j) + (k) * 4096, 1024)
    dma_m<0>(m0w2 + 0, v2[0], rs_w2, s2o[3] + 0 * W2ROW); WAIT_VM(26); RW(0, 0, 0); RW(0, 0, 1); SB();
    dma_n<1024>(v2[1], rs_w2, s2o[3] + 0 * W2ROW); RW(0, 0, 2); RW(0, 0, 3); SB();
    dma_n<2048>(v2[2], rs_w2, s2o[3] + 0 * W2ROW); RW(0, 1, 0); RW(0, 1, 1); SB();
    dma_n<3072>(v2[3], rs_w2, s2o[3] + 0 * W2ROW); RW(0, 1, 2); RW(0, 1, 3); SB();
    dma_m<0>(m0w2 + 4096, v2[0], rs_w2, s2o[3] + 32 * W2ROW); WAIT_VM(22); RW(1, 0, 0); RW(1, 0, 1); SB();
    dma_n<1024>(v2[1], rs_w2, s2o[3] + 32 * W2ROW); RW(1, 0, 2); RW(1, 0, 3); SB();
    dma_n<2048>(v2[2], rs_w2, s2o[3] + 32 * W2ROW); RW(1, 1, 0); RW(1, 1, 1); SB();
    dma_n<3072>(v2[3], rs_w2, s2o[3] + 32 * W2ROW); RW(1, 1, 2); RW(1, 1, 3); SB();
    STAMP(11);
    WAIT_VM(8);
    asm volatile("" : "+v"(nd));
    BARRIER();
    STAMP(5);
    if (wave == 0) st_agent(BOX_WR, nd);
    dma_m<0>(m0w2 + 32768, v2[0], rs_w2, s2o[4] + 0 * W2ROW); WAIT_VM(11); RW(2, 0, 0); RW(2, 0, 1); SB();
    dma_n<1024>(v2[1], rs_w2, s2o[4] + 0 * W2ROW); RW(2, 0, 2); RW(2, 0, 3); SB();
    dma_n<2048>(v2[2], rs_w2, s2o[4] + 0 * W2ROW); RW(2, 1, 0); RW(2, 1, 1); SB();
    dma_n<3072>(v2[3], rs_w2, s2o[4] + 0 * W2ROW); RW(2, 1, 2); RW(2, 1, 3); SB();
    dma_m<0>(m0w2 + 36864, v2[0], rs_w2, s2o[4] + 32 * W2ROW);  SB();
    dma_n<1024>(v2[1], rs_w2, s2o[4] + 32 * W2ROW);  SB();
    dma_n<2048>(v2[2], rs_w2, s2o[4] + 32 * W2ROW);  SB();
    dma_n<3072>(v2[3], rs_w2, s2o[4] + 32 * W2ROW); WAIT_VM(8); RW(3, 0, 0); RW(3, 0, 1); SB();
    WAIT_LGKM0();
    dma_m<0>(m0w2 + 131072, v2[0], rs_w2, s2o[5] + 0 * W2ROW); RW(3, 0, 2); RW(3, 0, 3); SB();
    dma_n<1024>(v2[1], rs_w2, s2o[5] + 0 * W2ROW); RW(3, 1, 0); RW(3, 1, 1); SB();
    dma_n<2048>(v2[2], rs_w2, s2o[5] + 0 * W2ROW); RW(3, 1, 2); RW(3, 1, 3); SB();
    dma_n<3072>(v2[3], rs_w2, s2o[5] + 0 * W2ROW);  SB();
    dma_m<0>(m0w2 + 135168, v2[0], rs_w2, s2o[5] + 32 * W2ROW);  SB();
    dma_n<1024>(v2[1], rs_w2, s2o[5] + 32 * W2ROW);  SB();
    dma_n<2048>(v2[2], rs_w2, s2o[5] + 32 * W2ROW);  SB();
    dma_n<3072>(v2[3], rs_w2, s2o[5] + 32 * W2ROW); WAIT_VM(8); RW(4, 0, 0); RW(4, 0, 1); SB();
    WAIT_LGKM0();
    dma_m<0>(m0w2 + 65536, v2[0], rs_w2, s2o[6] + 0 * W2ROW); RW(4, 0, 2); RW(4, 0, 3); SB();
    dma_n<1024>(v2[1], rs_w2, s2o[6] + 0 * W2ROW); RW(4, 1, 0); RW(4, 1, 1); SB();
    dma_n<2048>(v2[2], rs_w2, s2o[6] + 0 * W2ROW); RW(4, 1, 2); RW(4, 1, 3); SB();
    dma_n<3072>(v2[3], rs_w2, s2o[6] + 0 * W2ROW);  SB();
    dma_m<0>(m0w2 + 69632, v2[0], rs_w2, s2o[6] + 32 * W2ROW);  SB();
    dma_n<1024>(v2[1], rs_w2, s2o[6] + 32 * W2ROW);  SB();
    dma_n<2048>(v2[2], rs_w2, s2o[6] + 32 * W2ROW);  SB();
    dma_n<3072>(v2[3], rs_w2, s2o[6] + 32 * W2ROW); WAIT_VM(8); RW(5, 0, 0); RW(5, 0, 1); SB();
    RW(5, 0, 2); RW(5, 0, 3); RW(5, 1, 0); RW(5, 1, 1); RW(5, 1, 2); RW(5, 1, 3); WAIT_VM(0); RW(6, 0, 0); RW(6, 0, 1); SB();
    RW(6, 0, 2); RW(6, 0, 3); RW(6, 1, 0); RW(6, 1, 1); RW(6, 1, 2); RW(6, 1, 3); SB();
    WAIT_LGKM0();
  }
  STAMP(6);
  if (wave == 0) {
    for (unsigned spins = 0; spins < (1u << 22); ++spins) {
      if (__builtin_amdgcn_ballot_w64(ld_agent(BOX_RD) != nonce) == 0) break;
      __builtin_amdgcn_s_sleep(1);
    }
  }
  BARRIER();
  STAMP(7);
  unsigned nd2 = nd;
  if (wave == 0) nd2 = ld_agent(nonce_w + lane);

  {
    const auto rs_h = __builtin_amdgcn_make_buffer_rsrc((void*)ws, 0, H_BYTES, 0x00020000);
    const unsigned hb = ((unsigned)mb0 * KS2 + wave * 14) * 1024;
    const unsigned vo = lane * 16;
    const int rot2 = 0;
    constexpr int HD = HDIST, HR = HDIST + 1;
    u32x4 H[HR];
    auto load_h1 = [&](int t) {
      const int mb = t / 14, k = t % 14;
      int mr_ = mb + rot2; if (mr_ >= nmb) mr_ -= nmb;
      const unsigned so = mb < nmb ? hb + (unsigned)mr_ * (KS2 * 1024) : 0x40000000u;
      H[t % HR] = __builtin_amdgcn_raw_buffer_load_b128(rs_h, vo + (k & 1) * 1024, so + hko[k >> 1], 16);
    };
    CSTAMP2(0);
#pragma unroll
    for (int t = 0; t < HD; ++t) load_h1(t);
    SB();
    CSTAMP2(1);
    const int m = lane & 15, g = lane >> 4;
#pragma unroll
    for (int mb = 0; mb < 6; ++mb) {
      if (mb < 4 || mb < nmb) {
        f32x4 acc[4];
#pragma unroll
        for (int nb = 0; nb < 4; ++nb) acc[nb] = f32x4{0.f, 0.f, 0.f, 0.f};
#pragma unroll
        for (int k = 0; k < 14; ++k) {
          const int t = mb * 14 + k;
#pragma unroll
          for (int nb = 0; nb < 4; ++nb) {
            acc[nb] = __builtin_amdgcn_mfma_f32_16x16x32_bf16(W[k][nb], __builtin_bit_cast(bf16x8, H[t % HR]), acc[nb], 0, 0, 0);
            if (nb == 0 && t + HD < 84) { SB(); load_h1(t + HD); SB(); }
            if (t == 0 && nb == 0) { CSTAMP2(2); }
          }
        }
#pragma unroll
        for (int nb = 0; nb < 4; ++nb)
          *(LAS f32x4*)(smem + mb * 16384 + wave * 4096 + nb * 1024 + g * 256 + ((m ^ (4 * nb + (g & 2))) << 4)) = acc[nb];
        SB();
        if (mb < 4) { CSTAMP2(3 + mb); }
      }
    }
    WAIT_LGKM0();
    BARRIER();
    STAMP(8);
    const auto rs_o = __builtin_amdgcn_make_buffer_rsrc((void*)out, 0, OUT_BYTES, 0x00020000);
    const int c = lane & 7, nb = c >> 1;
#pragma unroll 1
    for (int u = wave; u < 2 * nmb; u += 4) {
      const int mb = u >> 1, mr = (u & 1) * 8 + (lane >> 3);
      LAS char* base = smem + mb * 16384 + nb * 1024 + (c & 1) * 512 + ((mr ^ (2 * c)) << 4);
      f32x4 lo = *(LAS f32x4*)base, hi4 = *(LAS f32x4*)(base + 256);
#pragma unroll
      for (int wv = 1; wv < 4; ++wv) { lo += *(LAS f32x4*)(base + wv * 4096); hi4 += *(LAS f32x4*)(base + wv * 4096 + 256); }
      u32x4 v;
      v[0] = pack_bf16(lo[0], lo[1]); v[1] = pack_bf16(lo[2], lo[3]); v[2] = pack_bf16(hi4[0], hi4[1]); v[3] = pack_bf16(hi4[2], hi4[3]);
      int mbr = mb + rot2; if (mbr >= nmb) mbr -= nmb;
      __builtin_amdgcn_raw_buffer_store_b128(v, rs_o, (unsigned)((mb0 + mbr) * 16 + mr) * (kD * 2) + c * 16, lc2 * 128, 16);
    }
  }
  if (wave == 0 && nd2 != nd) st_agent(BOX_WR, nd2);
  STAMP(9);
}

extern "C" void kernel_launch(void* const* d_in, const int* in_sizes, int n_in, void* d_out, int out_size, void* d_ws,
                              size_t ws_size, hipStream_t stream) {
  (void)in_sizes; (void)n_in; (void)out_size; (void)ws_size;
  hipFuncSetAttribute((const void*)ffn_relu_kernel, hipFuncAttributeMaxDynamicSharedMemorySize, LDS_BYTES);
  ffn_relu_kernel<<<256, 256, LDS_BYTES, stream>>>((const char*)d_in[0], (const char*)d_in[1], (const char*)d_in[2],
                                                   (char*)d_out, (char*)d_ws);
}
```

```cpp
#include <hip/hip_runtime.h>
#include <stdint.h>

typedef short bf16x8 __attribute__((ext_vector_type(8)));
typedef short s16x4 __attribute__((ext_vector_type(4)));
typedef float f32x4 __attribute__((ext_vector_type(4)));
typedef float f32x2 __attribute__((ext_vector_type(2)));
typedef float f32x16 __attribute__((ext_vector_type(16)));
typedef unsigned u32x4 __attribute__((ext_vector_type(4)));
typedef __bf16 bf16x2_t __attribute__((ext_vector_type(2)));
#define LAS __attribute__((address_space(3)))
#define GAS __attribute__((address_space(1)))
#define DI __device__ __forceinline__

#define HDIST 12
#define ROTW 5
#define PW 3

namespace {
constexpr int kM = 2048, kD = 640, kF = 1728;
constexpr unsigned X_BYTES = kM * kD * 2, W1_BYTES = kD * kF * 2, W2_BYTES = kF * kD * 2, OUT_BYTES = kM * kD * 2;
constexpr unsigned XROW = kD * 2, W1ROW = kF * 2, W2ROW = kD * 2;
constexpr int KS2 = kF / 32;
constexpr unsigned H_BYTES = (kM / 16) * KS2 * 1024;
constexpr unsigned SLOT = 32768, LDS_BYTES = 5 * SLOT;
constexpr size_t WS_NONCE = 8u << 20, WS_INBOX = WS_NONCE + 4096;

DI u32x4 mk_rsrc(const void* p, unsigned bytes) {
  uint64_t a = (uint64_t)p;
  u32x4 r;
  r[0] = (unsigned)a; r[1] = (unsigned)(a >> 32) & 0xffffu; r[2] = bytes; r[3] = 0x00020000u;
  return r;
}
template <int OFF>
DI void dma_m(unsigned m0v, unsigned va, u32x4 rs, unsigned soff) {
  asm volatile("s_mov_b32 m0, %0\n\ts_nop 0\n\t"
               "buffer_load_dwordx4 %1, %2, %3 offen offset:%4 lds"
               :: "s"(m0v), "v"(va), "s"(rs), "s"(soff), "i"(OFF) : "memory");
}
template <int OFF>
DI void dma_n(unsigned va, u32x4 rs, unsigned soff) {
  asm volatile("buffer_load_dwordx4 %0, %1, %2 offen offset:%3 lds" :: "v"(va), "s"(rs), "s"(soff), "i"(OFF) : "memory");
}
#define SB() __builtin_amdgcn_sched_barrier(0)
#define WAIT_VM(n) asm volatile("s_waitcnt vmcnt(" #n ")" ::: "memory")
#define WAIT_LGKM0() asm volatile("s_waitcnt lgkmcnt(0)" ::: "memory")
#define BARRIER() do { asm volatile("" ::: "memory"); __builtin_amdgcn_s_barrier(); asm volatile("" ::: "memory"); } while (0)

DI bf16x8 tr_read2(LAS char* p, int second_off) {
  s16x4 a = __builtin_amdgcn_ds_read_tr16_b64_v4i16((LAS s16x4*)p);
  s16x4 b = __builtin_amdgcn_ds_read_tr16_b64_v4i16((LAS s16x4*)(p + second_off));
  return __builtin_shufflevector(a, b, 0, 1, 2, 3, 4, 5, 6, 7);
}
DI unsigned pack_bf16(float a, float b) {
  f32x2 v = {a, b};
  return __builtin_bit_cast(unsigned, __builtin_convertvector(v, bf16x2_t));
}
DI float relu_nan(float v) { return __builtin_elementwise_maximum(v, 0.0f); }
DI unsigned ld_agent(const unsigned* p) { return __hip_atomic_load((const GAS unsigned*)p, __ATOMIC_RELAXED, __HIP_MEMORY_SCOPE_AGENT); }
DI void st_agent(unsigned* p, unsigned v) { __hip_atomic_store((GAS unsigned*)p, v, __ATOMIC_RELAXED, __HIP_MEMORY_SCOPE_AGENT); }

}

__global__ __launch_bounds__(256, 1) void ffn_relu_kernel(const char* __restrict__ x, const char* __restrict__ w1,
                                                          const char* __restrict__ w2, char* __restrict__ out,
                                                          char* __restrict__ ws) {
  extern __shared__ __attribute__((aligned(16))) char smem_g[];
  LAS char* smem = (LAS char*)smem_g;
  const unsigned lds0 = (unsigned)(size_t)smem;
  const int lane = threadIdx.x & 63;
  const int wave = __builtin_amdgcn_readfirstlane(threadIdx.x >> 6);
  const int b = blockIdx.x, xg = b & 7, idx = b >> 3;
  const int grp = xg >> 1, par = xg & 1;
  const int member = par * 32 + idx;

  asm volatile(".Lffn_kbeg:" :: "s"(x), "s"(w1), "s"(w2), "s"(out), "s"(ws));
  unsigned* nonce_w = (unsigned*)(ws + WS_NONCE) + grp * 64;
  unsigned* inbox = (unsigned*)(ws + WS_INBOX) + (size_t)grp * 64 * 64;
#define BOX_RD (inbox + lane * 64 + member)
#define BOX_WR (inbox + member * 64 + lane)
  unsigned in0 = 0;
#define LD_ASYNC(dst, p) asm volatile("global_load_dword %0, %1, off sc1" : "+v"(dst) : "v"(p) : "memory")
  if (wave == PW) LD_ASYNC(in0, BOX_RD);

  const bool has_p1 = (idx & 7) != 7;
  u32x4 cpf[6];
  if (!has_p1) {
    unsigned long long pc; unsigned fwd, back;
    asm volatile("s_getpc_b64 %0\n.Lffn_kmid:\n\ts_mov_b32 %1, .Lffn_kend-.Lffn_kmid\n\ts_mov_b32 %2, .Lffn_kmid-.Lffn_kbeg" : "=s"(pc), "=s"(fwd), "=s"(back));
    const u32x4 rc = mk_rsrc((const void*)(pc - back), fwd + back);
#pragma unroll
    for (int i = 0; i < 6; ++i)
      asm volatile("buffer_load_dwordx4 %0, %1, %2, %3 offen" : "=v"(cpf[i]) : "v"(lane * 16), "s"(rc), "s"((i * 4 + wave) * 1024) : "memory");
  }
  SB();
  const int pm = grp * 4 + (idx >> 3), pn = par * 7 + (idx & 7);
  const int col0 = pn == 13 ? kF - 128 : pn * 128;
  const u32x4 rs_x = mk_rsrc(x, X_BYTES), rs_w1 = mk_rsrc(w1, W1_BYTES);
  int kr = (pn + 3 * pm) % ROTW, kb = 0, kn = 0;
  auto next_k = [&]() {
    const int k = kb + kr;
    kr = kr == ROTW - 1 ? 0 : kr + 1;
    if (ROTW < 10) { ++kn; if (kn == ROTW) { kn = 0; kb += ROTW; } }
    return k;
  };
  unsigned vx[4], vw[4];
#pragma unroll
  for (int i = 0; i < 4; ++i) {
    vx[i] = (lane >> 3) * XROW + (((lane & 7) ^ ((4 * (i & 1) + (lane >> 4)) & 7)) << 4) + i * (8 * XROW - 1024);
    vw[i] = (lane >> 4) * W1ROW + (((lane & 15) ^ ((lane >> 4) << 2)) << 4) + i * (4 * W1ROW - 1024);
    if (pn == 13 && ((lane ^ (lane >> 2)) & 8) == 0) vw[i] = 0x80000000u;
  }
  const unsigned sx_base = (unsigned)(pm * 128 + wave * 32) * XROW, sw_base = (unsigned)(wave * 16) * W1ROW + col0 * 2;
  const unsigned m0base = lds0 + wave * 4096;
  int k0 = 0, k1 = 0, k2 = 0; unsigned sx0 = 0, sw0 = 0, sx1 = 0, sw1 = 0, sx2 = 0, sw2 = 0;
  (void)k2; (void)sx2; (void)sw2;
  if (has_p1) {
    k0 = next_k(); sx0 = sx_base + k0 * 128, sw0 = sw_base + (unsigned)k0 * (64 * W1ROW); dma_m<0>(m0base + 0, vx[0], rs_x, sx0);
    dma_n<1024>(vx[1], rs_x, sx0);
    dma_n<2048>(vx[2], rs_x, sx0);
    dma_n<3072>(vx[3], rs_x, sx0);
    dma_m<0>(m0base + 16384, vw[0], rs_w1, sw0);
    dma_n<1024>(vw[1], rs_w1, sw0);
    dma_n<2048>(vw[2], rs_w1, sw0);
    dma_n<3072>(vw[3], rs_w1, sw0);
    k1 = next_k(); sx1 = sx_base + k1 * 128, sw1 = sw_base + (unsigned)k1 * (64 * W1ROW); dma_m<0>(m0base + 32768, vx[0], rs_x, sx1);
    dma_n<1024>(vx[1], rs_x, sx1);
    dma_n<2048>(vx[2], rs_x, sx1);
    dma_n<3072>(vx[3], rs_x, sx1);
  }
  SB();

  int strip, pos, nmb, mb0;
  if (idx < 14) { strip = idx >= 7; pos = idx - 7 * strip; nmb = pos < 4 ? 5 : 4; mb0 = pos < 4 ? 5 * pos : 4 + 4 * pos; }
  else { int j = idx - 14; strip = 2 + j / 6; pos = j % 6; nmb = pos < 4 ? 5 : 6; mb0 = pos < 4 ? 5 * pos : 6 * pos - 4; }
  mb0 += grp * 32;
  int lc2 = par * 5 + strip;

  const u32x4 rs_w2 = mk_rsrc(w2, W2_BYTES);
  unsigned v2[4];
#pragma unroll
  for (int i = 0; i < 4; ++i)
    v2[i] = (lane >> 3) * W2ROW + (((lane & 7) ^ ((((lane >> 4) & 1) | (((i >> 1) & 1) << 1)) << 1)) << 4) + i * (8 * W2ROW - 1024);
  const unsigned s2_base = (unsigned)(wave * 448) * W2ROW + lc2 * 128;
#define W2SLOT(j) ((j) == 0 || (j) == 5 ? 4 * SLOT : (j) == 1 || (j) == 6 ? 2 * SLOT : (j) == 2 ? 3 * SLOT : (j) == 3 ? 0u : SLOT)
#define PICK_NONCE() do { if (wave == PW) { asm volatile("" : "+v"(in0)); \
    nonce = __builtin_amdgcn_readfirstlane(in0) + 1u; \
    while (__builtin_amdgcn_ballot_w64(in0 == nonce) != 0) ++nonce; \
    if (lane == 0) st_agent(nonce_w + member, nonce); } } while (0)
#define LOAD_ND() do { if (wave == PW) LD_ASYNC(nd, nonce_w + lane); } while (0)
  const unsigned m0w2 = lds0 + wave * 8192;
  unsigned s2o[7], hko[7];
#pragma unroll
  for (int j = 0; j < 7; ++j) {
    const int jr = j;
    s2o[j] = s2_base + (unsigned)jr * (64 * W2ROW);
    hko[j] = (wave == 3 && jr == 6) ? 0x80000000u : (unsigned)jr * 2048;
  }
  unsigned nonce = 0, nd = 0;
  if (has_p1) {
    const int wm = wave >> 1, wf = wave & 1;
    const int hi = lane >> 5, r31 = lane & 31;
    LAS char* xr[4][2]; LAS char* wr[2][2];
#define SETUP_READ() do { \
    const unsigned xrow = wm * 64 + r31; \
    const unsigned xa0 = xrow * 128 + ((hi ^ ((xrow >> 1) & 7)) << 4); \
    const int fh = (lane >> 4) & 1, q = (lane & 15) >> 2, p = lane & 3; \
    const unsigned wa0 = 16384 + (8 * hi + q) * 256 + (((8 * wf + 2 * fh + (p >> 1)) ^ (q << 2)) << 4) + 8 * (p & 1); \
    _Pragma("unroll") for (int h = 0; h < 2; ++h) { \
      _Pragma("unroll") for (int k = 0; k < 4; ++k) { xr[k][h] = smem + (xa0 ^ (k << 5)) + h * 65536; asm volatile("" : "+v"(xr[k][h])); } \
      wr[0][h] = smem + wa0 + h * 65536; wr[1][h] = smem + (wa0 ^ 64) + h * 65536; asm volatile("" : "+v"(wr[0][h]), "+v"(wr[1][h])); \
    } } while (0)
    f32x16 acc[2][2];
    bf16x8 A[8][2], B[8][2];
#define MF0(i, j, a, b) asm volatile("v_mfma_f32_32x32x16_bf16 %0, %1, %2, 0" : "=&v"(acc[i][j]) : "v"(a), "v"(b))
#define MF1(i, j, a, b) asm volatile("v_mfma_f32_32x32x16_bf16 %0, %1, %2, %0" : "+v"(acc[i][j]) : "v"(a), "v"(b))
    const auto rs_h = __builtin_amdgcn_make_buffer_rsrc((void*)ws, 0, H_BYTES, 0x00020000);
    const unsigned dead = (pn == 13 && wf == 0) ? 0x80000000u : 0u;
    const unsigned hs_base = ((unsigned)(pm * 8 + wm * 4) * KS2 + (col0 >> 5) + 2 * wf) * 1024 + dead;
    const unsigned hs_vo = (r31 >> 4) * (KS2 * 1024) + hi * 512 + (lane & 15) * 16;
    u32x4 ev[2];
#define EPI_WORD(fb, mb, s, e) ev[s][e] = pack_bf16(relu_nan(acc[fb][mb][8 * (s) + 2 * (e)]), relu_nan(acc[fb][mb][8 * (s) + 2 * (e) + 1]))
#define EPI_STORE(fb, mb, s) do { __builtin_amdgcn_raw_buffer_store_b128(ev[s], rs_h, hs_vo + (s) * 256, hs_base + ((mb) * 2 * KS2 + (fb)) * 1024, 16); \
      SB(); asm volatile("s_nop 1"); } while (0)
    SETUP_READ();
    SB();
    dma_m<0>(m0base + 49152, vw[0], rs_w1, sw1);
    dma_n<1024>(vw[1], rs_w1, sw1);
    dma_n<2048>(vw[2], rs_w1, sw1);
    dma_n<3072>(vw[3], rs_w1, sw1);
    WAIT_VM(16);
    PICK_NONCE();
    WAIT_VM(8); BARRIER();
    B[0][0] = *(LAS bf16x8*)(xr[0][0] + 0); B[0][1] = *(LAS bf16x8*)(xr[0][0] + 4096); A[0][0] = tr_read2(wr[0][0] + 0, 1024); A[0][1] = tr_read2(wr[1][0] + 0, 1024); B[1][0] = *(LAS bf16x8*)(xr[1][0] + 0); B[1][1] = *(LAS bf16x8*)(xr[1][0] + 4096); A[1][0] = tr_read2(wr[0][0] + 4096, 1024); A[1][1] = tr_read2(wr[1][0] + 4096, 1024); SB();
    MF0(0, 0, A[0][0], B[0][0]); SB();
    B[2][0] = *(LAS bf16x8*)(xr[2][0] + 0); B[2][1] = *(LAS bf16x8*)(xr[2][0] + 4096); const int k2 = next_k(); const unsigned sx2 = sx_base + k2 * 128, sw2 = sw_base + (unsigned)k2 * (64 * W1ROW); dma_m<0>(m0base + 65536, vx[0], rs_x, sx2); SB();
    MF0(0, 1, A[0][0], B[0][1]); SB();
    A[2][0] = tr_read2(wr[0][0] + 8192, 1024); A[2][1] = tr_read2(wr[1][0] + 8192, 1024); SB();
    MF0(1, 1, A[0][1], B[0][1]); SB();
    dma_n<1024>(vx[1], rs_x, sx2); SB();
    MF0(1, 0, A[0][1], B[0][0]); SB();
    dma_n<2048>(vx[2], rs_x, sx2); SB();
    MF1(0, 0, A[1][0], B[1][0]); SB();
    B[3][0] = *(LAS bf16x8*)(xr[3][0] + 0); B[3][1] = *(LAS bf16x8*)(xr[3][0] + 4096); dma_n<3072>(vx[3], rs_x, sx2); SB();
    MF1(0, 1, A[1][0], B[1][1]); SB();
    A[3][0] = tr_read2(wr[0][0] + 12288, 1024); A[3][1] = tr_read2(wr[1][0] + 12288, 1024); SB();
    MF1(1, 1, A[1][1], B[1][1]); SB();
    dma_m<0>(m0base + 81920, vw[0], rs_w1, sw2); SB();
    MF1(1, 0, A[1][1], B[1][0]); SB();
    WAIT_LGKM0(); WAIT_VM(5); BARRIER(); SB();
    MF1(0, 0, A[2][0], B[2][0]); SB();
    B[4][0] = *(LAS bf16x8*)(xr[0][0] + 32768); B[4][1] = *(LAS bf16x8*)(xr[0][0] + 36864); dma_n<1024>(vw[1], rs_w1, sw2); SB();
    MF1(0, 1, A[2][0], B[2][1]); SB();
    A[4][0] = tr_read2(wr[0][0] + 32768, 1024); A[4][1] = tr_read2(wr[1][0] + 32768, 1024); SB();
    MF1(1, 1, A[2][1], B[2][1]); SB();
    dma_n<2048>(vw[2], rs_w1, sw2); SB();
    MF1(1, 0, A[2][1], B[2][0]); SB();
    dma_n<3072>(vw[3], rs_w1, sw2); SB();
    MF1(0, 0, A[3][0], B[3][0]); SB();
    B[5][0] = *(LAS bf16x8*)(xr[1][0] + 32768); B[5][1] = *(LAS bf16x8*)(xr[1][0] + 36864); const int k3 = next_k(); const unsigned sx3 = sx_base + k3 * 128, sw3 = sw_base + (unsigned)k3 * (64 * W1ROW); dma_m<0>(m0base + 98304, vx[0], rs_x, sx3); SB();
    MF1(0, 1, A[3][0], B[3][1]); SB();
    A[5][0] = tr_read2(wr[0][0] + 36864, 1024); A[5][1] = tr_read2(wr[1][0] + 36864, 1024); SB();
    MF1(1, 1, A[3][1], B[3][1]); SB();
    dma_n<1024>(vx[1], rs_x, sx3); SB();
    MF1(1, 0, A[3][1], B[3][0]); SB();
    dma_n<2048>(vx[2], rs_x, sx3); SB();
    MF1(0, 0, A[4][0], B[4][0]); SB();
    B[6][0] = *(LAS bf16x8*)(xr[2][0] + 32768); B[6][1] = *(LAS bf16x8*)(xr[2][0] + 36864); dma_n<3072>(vx[3], rs_x, sx3); SB();
    MF1(0, 1, A[4][0], B[4][1]); SB();
    A[6][0] = tr_read2(wr[0][0] + 40960, 1024); A[6][1] = tr_read2(wr[1][0] + 40960, 1024); SB();
    MF1(1, 1, A[4][1], B[4][1]); SB();
    dma_m<0>(m0base + 114688, vw[0], rs_w1, sw3); SB();
    MF1(1, 0, A[4][1], B[4][0]); SB();
    dma_n<1024>(vw[1], rs_w1, sw3); SB();
    MF1(0, 0, A[5][0], B[5][0]); SB();
    B[7][0] = *(LAS bf16x8*)(xr[3][0] + 32768); B[7][1] = *(LAS bf16x8*)(xr[3][0] + 36864); dma_n<2048>(vw[2], rs_w1, sw3); SB();
    MF1(0, 1, A[5][0], B[5][1]); SB();
    A[7][0] = tr_read2(wr[0][0] + 45056, 1024); A[7][1] = tr_read2(wr[1][0] + 45056, 1024); SB();
    MF1(1, 1, A[5][1], B[5][1]); SB();
    SB();
    MF1(1, 0, A[5][1], B[5][0]); SB();
    WAIT_LGKM0(); WAIT_VM(7); BARRIER(); SB();
    MF1(0, 0, A[6][0], B[6][0]); SB();
    B[0][0] = *(LAS bf16x8*)(xr[0][1] + 0); B[0][1] = *(LAS bf16x8*)(xr[0][1] + 4096); dma_n<3072>(vw[3], rs_w1, sw3); SB();
    MF1(0, 1, A[6][0], B[6][1]); SB();
    A[0][0] = tr_read2(wr[0][1] + 0, 1024); A[0][1] = tr_read2(wr[1][1] + 0, 1024); SB();
    MF1(1, 1, A[6][1], B[6][1]); SB();
    const int k4 = next_k(); const unsigned sx4 = sx_base + k4 * 128, sw4 = sw_base + (unsigned)k4 * (64 * W1ROW); dma_m<0>(m0base + 0, vx[0], rs_x, sx4); SB();
    MF1(1, 0, A[6][1], B[6][0]); SB();
    dma_n<1024>(vx[1], rs_x, sx4); SB();
    MF1(0, 0, A[7][0], B[7][0]); SB();
    B[1][0] = *(LAS bf16x8*)(xr[1][1] + 0); B[1][1] = *(LAS bf16x8*)(xr[1][1] + 4096); dma_n<2048>(vx[2], rs_x, sx4); SB();
    MF1(0, 1, A[7][0], B[7][1]); SB();
    A[1][0] = tr_read2(wr[0][1] + 4096, 1024); A[1][1] = tr_read2(wr[1][1] + 4096, 1024); SB();
    MF1(1, 1, A[7][1], B[7][1]); SB();
    dma_n<3072>(vx[3], rs_x, sx4); SB();
    MF1(1, 0, A[7][1], B[7][0]); SB();
    dma_m<0>(m0base + 16384, vw[0], rs_w1, sw4); SB();
    MF1(0, 0, A[0][0], B[0][0]); SB();
    B[2][0] = *(LAS bf16x8*)(xr[2][1] + 0); B[2][1] = *(LAS bf16x8*)(xr[2][1] + 4096); dma_n<1024>(vw[1], rs_w1, sw4); SB();
    MF1(0, 1, A[0][0], B[0][1]); SB();
    A[2][0] = tr_read2(wr[0][1] + 8192, 1024); A[2][1] = tr_read2(wr[1][1] + 8192, 1024); SB();
    MF1(1, 1, A[0][1], B[0][1]); SB();
    dma_n<2048>(vw[2], rs_w1, sw4); SB();
    MF1(1, 0, A[0][1], B[0][0]); SB();
    dma_n<3072>(vw[3], rs_w1, sw4); SB();
    MF1(0, 0, A[1][0], B[1][0]); SB();
    B[3][0] = *(LAS bf16x8*)(xr[3][1] + 0); B[3][1] = *(LAS bf16x8*)(xr[3][1] + 4096); const int k5 = next_k(); const unsigned sx5 = sx_base + k5 * 128, sw5 = sw_base + (unsigned)k5 * (64 * W1ROW); dma_m<0>(m0base + 32768, vx[0], rs_x, sx5); SB();
    MF1(0, 1, A[1][0], B[1][1]); SB();
    A[3][0] = tr_read2(wr[0][1] + 12288, 1024); A[3][1] = tr_read2(wr[1][1] + 12288, 1024); SB();
    MF1(1, 1, A[1][1], B[1][1]); SB();
    MF1(1, 0, A[1][1], B[1][0]); SB();
    WAIT_LGKM0(); WAIT_VM(9); BARRIER(); SB();
    MF1(0, 0, A[2][0], B[2][0]); SB();
    B[4][0] = *(LAS bf16x8*)(xr[0][1] + 32768); B[4][1] = *(LAS bf16x8*)(xr[0][1] + 36864); dma_n<1024>(vx[1], rs_x, sx5); SB();
    MF1(0, 1, A[2][0], B[2][1]); SB();
    A[4][0] = tr_read2(wr[0][1] + 32768, 1024); A[4][1] = tr_read2(wr[1][1] + 32768, 1024); SB();
    MF1(1, 1, A[2][1], B[2][1]); SB();
    dma_n<2048>(vx[2], rs_x, sx5); SB();
    MF1(1, 0, A[2][1], B[2][0]); SB();
    dma_n<3072>(vx[3], rs_x, sx5); SB();
    MF1(0, 0, A[3][0], B[3][0]); SB();
    B[5][0] = *(LAS bf16x8*)(xr[1][1] + 32768); B[5][1] = *(LAS bf16x8*)(xr[1][1] + 36864); dma_m<0>(m0base + 49152, vw[0], rs_w1, sw5); SB();
    MF1(0, 1, A[3][0], B[3][1]); SB();
    A[5][0] = tr_read2(wr[0][1] + 36864, 1024); A[5][1] = tr_read2(wr[1][1] + 36864, 1024); SB();
    MF1(1, 1, A[3][1], B[3][1]); SB();
    dma_n<1024>(vw[1], rs_w1, sw5); SB();
    MF1(1, 0, A[3][1], B[3][0]); SB();
    dma_n<2048>(vw[2], rs_w1, sw5); SB();
    MF1(0, 0, A[4][0], B[4][0]); SB();
    B[6][0] = *(LAS bf16x8*)(xr[2][1] + 32768); B[6][1] = *(LAS bf16x8*)(xr[2][1] + 36864); dma_n<3072>(vw[3], rs_w1, sw5); SB();
    MF1(0, 1, A[4][0], B[4][1]); SB();
    A[6][0] = tr_read2(wr[0][1] + 40960, 1024); A[6][1] = tr_read2(wr[1][1] + 40960, 1024); SB();
    MF1(1, 1, A[4][1], B[4][1]); SB();
    const int k6 = next_k(); const unsigned sx6 = sx_base + k6 * 128, sw6 = sw_base + (unsigned)k6 * (64 * W1ROW); dma_m<0>(m0base + 65536, vx[0], rs_x, sx6); SB();
    MF1(1, 0, A[4][1], B[4][0]); SB();
    dma_n<1024>(vx[1], rs_x, sx6); SB();
    MF1(0, 0, A[5][0], B[5][0]); SB();
    B[7][0] = *(LAS bf16x8*)(xr[3][1] + 32768); B[7][1] = *(LAS bf16x8*)(xr[3][1] + 36864); dma_n<2048>(vx[2], rs_x, sx6); SB();
    MF1(0, 1, A[5][0], B[5][1]); SB();
    A[7][0] = tr_read2(wr[0][1] + 45056, 1024); A[7][1] = tr_read2(wr[1][1] + 45056, 1024); SB();
    MF1(1, 1, A[5][1], B[5][1]); SB();
    MF1(1, 0, A[5][1], B[5][0]); SB();
    WAIT_LGKM0(); WAIT_VM(11); BARRIER(); SB();
    MF1(0, 0, A[6][0], B[6][0]); SB();
    B[0][0] = *(LAS bf16x8*)(xr[0][0] + 0); B[0][1] = *(LAS bf16x8*)(xr[0][0] + 4096); dma_n<3072>(vx[3], rs_x, sx6); SB();
    MF1(0, 1, A[6][0], B[6][1]); SB();
    A[0][0] = tr_read2(wr[0][0] + 0, 1024); A[0][1] = tr_read2(wr[1][0] + 0, 1024); SB();
    MF1(1, 1, A[6][1], B[6][1]); SB();
    dma_m<0>(m0base + 81920, vw[0], rs_w1, sw6); SB();
    MF1(1, 0, A[6][1], B[6][0]); SB();
    dma_n<1024>(vw[1], rs_w1, sw6); SB();
    MF1(0, 0, A[7][0], B[7][0]); SB();
    B[1][0] = *(LAS bf16x8*)(xr[1][0] + 0); B[1][1] = *(LAS bf16x8*)(xr[1][0] + 4096); dma_n<2048>(vw[2], rs_w1, sw6); SB();
    MF1(0, 1, A[7][0], B[7][1]); SB();
    A[1][0] = tr_read2(wr[0][0] + 4096, 1024); A[1][1] = tr_read2(wr[1][0] + 4096, 1024); SB();
    MF1(1, 1, A[7][1], B[7][1]); SB();
    dma_n<3072>(vw[3], rs_w1, sw6); SB();
    MF1(1, 0, A[7][1], B[7][0]); SB();
    const int k7 = next_k(); const unsigned sx7 = sx_base + k7 * 128, sw7 = sw_base + (unsigned)k7 * (64 * W1ROW); dma_m<0>(m0base + 98304, vx[0], rs_x, sx7); SB();
    MF1(0, 0, A[0][0], B[0][0]); SB();
    B[2][0] = *(LAS bf16x8*)(xr[2][0] + 0); B[2][1] = *(LAS bf16x8*)(xr[2][0] + 4096); dma_n<1024>(vx[1], rs_x, sx7); SB();
    MF1(0, 1, A[0][0], B[0][1]); SB();
    A[2][0] = tr_read2(wr[0][0] + 8192, 1024); A[2][1] = tr_read2(wr[1][0] + 8192, 1024); SB();
    MF1(1, 1, A[0][1], B[0][1]); SB();
    dma_n<2048>(vx[2], rs_x, sx7); SB();
    MF1(1, 0, A[0][1], B[0][0]); SB();
    dma_n<3072>(vx[3], rs_x, sx7); SB();
    MF1(0, 0, A[1][0], B[1][0]); SB();
    B[3][0] = *(LAS bf16x8*)(xr[3][0] + 0); B[3][1] = *(LAS bf16x8*)(xr[3][0] + 4096); dma_m<0>(m0base + 114688, vw[0], rs_w1, sw7); SB();
    MF1(0, 1, A[1][0], B[1][1]); SB();
    A[3][0] = tr_read2(wr[0][0] + 12288, 1024); A[3][1] = tr_read2(wr[1][0] + 12288, 1024); SB();
    MF1(1, 1, A[1][1], B[1][1]); SB();
    MF1(1, 0, A[1][1], B[1][0]); SB();
    WAIT_LGKM0(); WAIT_VM(13); BARRIER(); SB();
    MF1(0, 0, A[2][0], B[2][0]); SB();
    B[4][0] = *(LAS bf16x8*)(xr[0][0] + 32768); B[4][1] = *(LAS bf16x8*)(xr[0][0] + 36864); dma_n<1024>(vw[1], rs_w1, sw7); SB();
    MF1(0, 1, A[2][0], B[2][1]); SB();
    A[4][0] = tr_read2(wr[0][0] + 32768, 1024); A[4][1] = tr_read2(wr[1][0] + 32768, 1024); SB();
    MF1(1, 1, A[2][1], B[2][1]); SB();
    dma_n<2048>(vw[2], rs_w1, sw7); SB();
    MF1(1, 0, A[2][1], B[2][0]); SB();
    dma_n<3072>(vw[3], rs_w1, sw7); SB();
    MF1(0, 0, A[3][0], B[3][0]); SB();
    B[5][0] = *(LAS bf16x8*)(xr[1][0] + 32768); B[5][1] = *(LAS bf16x8*)(xr[1][0] + 36864); const int k8 = next_k(); const unsigned sx8 = sx_base + k8 * 128, sw8 = sw_base + (unsigned)k8 * (64 * W1ROW); dma_m<0>(m0base + 0, vx[0], rs_x, sx8); SB();
    MF1(0, 1, A[3][0], B[3][1]); SB();
    A[5][0] = tr_read2(wr[0][0] + 36864, 1024); A[5][1] = tr_read2(wr[1][0] + 36864, 1024); SB();
    MF1(1, 1, A[3][1], B[3][1]); SB();
    dma_n<1024>(vx[1], rs_x, sx8); SB();
    MF1(1, 0, A[3][1], B[3][0]); SB();
    dma_n<2048>(vx[2], rs_x, sx8); SB();
    MF1(0, 0, A[4][0], B[4][0]); SB();
    B[6][0] = *(LAS bf16x8*)(xr[2][0] + 32768); B[6][1] = *(LAS bf16x8*)(xr[2][0] + 36864); dma_n<3072>(vx[3], rs_x, sx8); SB();
    MF1(0, 1, A[4][0], B[4][1]); SB();
    A[6][0] = tr_read2(wr[0][0] + 40960, 1024); A[6][1] = tr_read2(wr[1][0] + 40960, 1024); SB();
    MF1(1, 1, A[4][1], B[4][1]); SB();
    dma_m<0>(m0base + 16384, vw[0], rs_w1, sw8); SB();
    MF1(1, 0, A[4][1], B[4][0]); SB();
    dma_n<1024>(vw[1], rs_w1, sw8); SB();
    MF1(0, 0, A[5][0], B[5][0]); SB();
    B[7][0] = *(LAS bf16x8*)(xr[3][0] + 32768); B[7][1] = *(LAS bf16x8*)(xr[3][0] + 36864); dma_n<2048>(vw[2], rs_w1, sw8); SB();
    MF1(0, 1, A[5][0], B[5][1]); SB();
    A[7][0] = tr_read2(wr[0][0] + 45056, 1024); A[7][1] = tr_read2(wr[1][0] + 45056, 1024); SB();
    MF1(1, 1, A[5][1], B[5][1]); SB();
    MF1(1, 0, A[5][1], B[5][0]); SB();
    WAIT_LGKM0(); WAIT_VM(15); BARRIER(); SB();
    MF1(0, 0, A[6][0], B[6][0]); SB();
    B[0][0] = *(LAS bf16x8*)(xr[0][1] + 0); B[0][1] = *(LAS bf16x8*)(xr[0][1] + 4096); dma_n<3072>(vw[3], rs_w1, sw8); SB();
    MF1(0, 1, A[6][0], B[6][1]); SB();
    A[0][0] = tr_read2(wr[0][1] + 0, 1024); A[0][1] = tr_read2(wr[1][1] + 0, 1024); SB();
    MF1(1, 1, A[6][1], B[6][1]); SB();
    const int k9 = next_k(); const unsigned sx9 = sx_base + k9 * 128, sw9 = sw_base + (unsigned)k9 * (64 * W1ROW); dma_m<0>(m0base + 32768, vx[0], rs_x, sx9); SB();
    MF1(1, 0, A[6][1], B[6][0]); SB();
    dma_n<1024>(vx[1], rs_x, sx9); SB();
    MF1(0, 0, A[7][0], B[7][0]); SB();
    B[1][0] = *(LAS bf16x8*)(xr[1][1] + 0); B[1][1] = *(LAS bf16x8*)(xr[1][1] + 4096); dma_n<2048>(vx[2], rs_x, sx9); SB();
    MF1(0, 1, A[7][0], B[7][1]); SB();
    A[1][0] = tr_read2(wr[0][1] + 4096, 1024); A[1][1] = tr_read2(wr[1][1] + 4096, 1024); SB();
    MF1(1, 1, A[7][1], B[7][1]); SB();
    dma_n<3072>(vx[3], rs_x, sx9); SB();
    MF1(1, 0, A[7][1], B[7][0]); SB();
    dma_m<0>(m0base + 49152, vw[0], rs_w1, sw9); SB();
    MF1(0, 0, A[0][0], B[0][0]); SB();
    B[2][0] = *(LAS bf16x8*)(xr[2][1] + 0); B[2][1] = *(LAS bf16x8*)(xr[2][1] + 4096); dma_n<1024>(vw[1], rs_w1, sw9); SB();
    MF1(0, 1, A[0][0], B[0][1]); SB();
    A[2][0] = tr_read2(wr[0][1] + 8192, 1024); A[2][1] = tr_read2(wr[1][1] + 8192, 1024); SB();
    MF1(1, 1, A[0][1], B[0][1]); SB();
    dma_n<2048>(vw[2], rs_w1, sw9); SB();
    MF1(1, 0, A[0][1], B[0][0]); SB();
    dma_n<3072>(vw[3], rs_w1, sw9); SB();
    MF1(0, 0, A[1][0], B[1][0]); SB();
    B[3][0] = *(LAS bf16x8*)(xr[3][1] + 0); B[3][1] = *(LAS bf16x8*)(xr[3][1] + 4096); dma_m<0>(m0w2 + 131072, v2[0], rs_w2, s2o[0] + 0 * W2ROW); SB();
    MF1(0, 1, A[1][0], B[1][1]); SB();
    A[3][0] = tr_read2(wr[0][1] + 12288, 1024); A[3][1] = tr_read2(wr[1][1] + 12288, 1024); SB();
    MF1(1, 1, A[1][1], B[1][1]); SB();
    MF1(1, 0, A[1][1], B[1][0]); SB();
    WAIT_LGKM0(); WAIT_VM(17); BARRIER(); SB();
    MF1(0, 0, A[2][0], B[2][0]); SB();
    B[4][0] = *(LAS bf16x8*)(xr[0][1] + 32768); B[4][1] = *(LAS bf16x8*)(xr[0][1] + 36864); dma_n<1024>(v2[1], rs_w2, s2o[0] + 0 * W2ROW); SB();
    MF1(0, 1, A[2][0], B[2][1]); SB();
    A[4][0] = tr_read2(wr[0][1] + 32768, 1024); A[4][1] = tr_read2(wr[1][1] + 32768, 1024); SB();
    MF1(1, 1, A[2][1], B[2][1]); SB();
    dma_n<2048>(v2[2], rs_w2, s2o[0] + 0 * W2ROW); SB();
    MF1(1, 0, A[2][1], B[2][0]); SB();
    dma_n<3072>(v2[3], rs_w2, s2o[0] + 0 * W2ROW); SB();
    MF1(0, 0, A[3][0], B[3][0]); SB();
    B[5][0] = *(LAS bf16x8*)(xr[1][1] + 32768); B[5][1] = *(LAS bf16x8*)(xr[1][1] + 36864); dma_m<0>(m0w2 + 135168, v2[0], rs_w2, s2o[0] + 32 * W2ROW); SB();
    MF1(0, 1, A[3][0], B[3][1]); SB();
    A[5][0] = tr_read2(wr[0][1] + 36864, 1024); A[5][1] = tr_read2(wr[1][1] + 36864, 1024); SB();
    MF1(1, 1, A[3][1], B[3][1]); SB();
    dma_n<1024>(v2[1], rs_w2, s2o[0] + 32 * W2ROW); SB();
    MF1(1, 0, A[3][1], B[3][0]); SB();
    dma_n<2048>(v2[2], rs_w2, s2o[0] + 32 * W2ROW); SB();
    MF1(0, 0, A[4][0], B[4][0]); SB();
    B[6][0] = *(LAS bf16x8*)(xr[2][1] + 32768); B[6][1] = *(LAS bf16x8*)(xr[2][1] + 36864); dma_n<3072>(v2[3], rs_w2, s2o[0] + 32 * W2ROW); SB();
    MF1(0, 1, A[4][0], B[4][1]); SB();
    A[6][0] = tr_read2(wr[0][1] + 40960, 1024); A[6][1] = tr_read2(wr[1][1] + 40960, 1024); SB();
    MF1(1, 1, A[4][1], B[4][1]); SB();
    dma_m<0>(m0w2 + 65536, v2[0], rs_w2, s2o[1] + 0 * W2ROW); SB();
    MF1(1, 0, A[4][1], B[4][0]); SB();
    dma_n<1024>(v2[1], rs_w2, s2o[1] + 0 * W2ROW); SB();
    MF1(0, 0, A[5][0], B[5][0]); SB();
    B[7][0] = *(LAS bf16x8*)(xr[3][1] + 32768); B[7][1] = *(LAS bf16x8*)(xr[3][1] + 36864); dma_n<2048>(v2[2], rs_w2, s2o[1] + 0 * W2ROW); SB();
    MF1(0, 1, A[5][0], B[5][1]); SB();
    A[7][0] = tr_read2(wr[0][1] + 45056, 1024); A[7][1] = tr_read2(wr[1][1] + 45056, 1024); SB();
    MF1(1, 1, A[5][1], B[5][1]); SB();
    MF1(1, 0, A[5][1], B[5][0]); SB();
    WAIT_LGKM0(); WAIT_VM(19); BARRIER(); SB();
    MF1(0, 0, A[6][0], B[6][0]); SB();
    B[0][0] = *(LAS bf16x8*)(xr[0][0] + 0); B[1][0] = *(LAS bf16x8*)(xr[1][0] + 0); dma_n<3072>(v2[3], rs_w2, s2o[1] + 0 * W2ROW); SB();
    MF1(0, 1, A[6][0], B[6][1]); SB();
    A[0][0] = tr_read2(wr[0][0] + 0, 1024); A[1][0] = tr_read2(wr[0][0] + 4096, 1024); SB();
    MF1(1, 1, A[6][1], B[6][1]); SB();
    dma_m<0>(m0w2 + 69632, v2[0], rs_w2, s2o[1] + 32 * W2ROW); SB();
    MF1(1, 0, A[6][1], B[6][0]); SB();
    dma_n<1024>(v2[1], rs_w2, s2o[1] + 32 * W2ROW); SB();
    MF1(0, 0, A[7][0], B[7][0]); SB();
    B[2][0] = *(LAS bf16x8*)(xr[2][0] + 0); B[3][0] = *(LAS bf16x8*)(xr[3][0] + 0); dma_n<2048>(v2[2], rs_w2, s2o[1] + 32 * W2ROW); SB();
    MF1(0, 1, A[7][0], B[7][1]); SB();
    A[2][0] = tr_read2(wr[0][0] + 8192, 1024); A[3][0] = tr_read2(wr[0][0] + 12288, 1024); SB();
    MF1(1, 1, A[7][1], B[7][1]); SB();
    dma_n<3072>(v2[3], rs_w2, s2o[1] + 32 * W2ROW); SB();
    MF1(1, 0, A[7][1], B[7][0]); SB();
    dma_m<0>(m0w2 + 98304, v2[0], rs_w2, s2o[2] + 0 * W2ROW); SB();
    MF1(0, 0, A[0][0], B[0][0]); SB();
    B[0][1] = *(LAS bf16x8*)(xr[0][0] + 4096); B[1][1] = *(LAS bf16x8*)(xr[1][0] + 4096); dma_n<1024>(v2[1], rs_w2, s2o[2] + 0 * W2ROW); SB();
    MF1(0, 0, A[1][0], B[1][0]); SB();
    B[2][1] = *(LAS bf16x8*)(xr[2][0] + 4096); B[3][1] = *(LAS bf16x8*)(xr[3][0] + 4096); dma_n<2048>(v2[2], rs_w2, s2o[2] + 0 * W2ROW); SB();
    MF1(0, 0, A[2][0], B[2][0]); SB();
    WAIT_VM(19); BARRIER(); SB();
    MF1(0, 0, A[3][0], B[3][0]); SB();
    B[4][0] = *(LAS bf16x8*)(xr[0][0] + 32768); A[4][0] = tr_read2(wr[0][0] + 32768, 1024); dma_n<3072>(v2[3], rs_w2, s2o[2] + 0 * W2ROW); SB();
    MF1(0, 1, A[0][0], B[0][1]); SB();
    B[5][0] = *(LAS bf16x8*)(xr[1][0] + 32768); A[5][0] = tr_read2(wr[0][0] + 36864, 1024); dma_m<0>(m0w2 + 102400, v2[0], rs_w2, s2o[2] + 32 * W2ROW); SB();
    MF1(0, 1, A[1][0], B[1][1]); SB();
    B[6][0] = *(LAS bf16x8*)(xr[2][0] + 32768); A[6][0] = tr_read2(wr[0][0] + 40960, 1024); SB();
    MF1(0, 1, A[2][0], B[2][1]); SB();
    B[7][0] = *(LAS bf16x8*)(xr[3][0] + 32768); A[7][0] = tr_read2(wr[0][0] + 45056, 1024); SB();
    MF1(0, 1, A[3][0], B[3][1]); SB();
    B[4][1] = *(LAS bf16x8*)(xr[0][0] + 36864); B[5][1] = *(LAS bf16x8*)(xr[1][0] + 36864); SB();
    MF1(0, 0, A[4][0], B[4][0]); SB();
    B[6][1] = *(LAS bf16x8*)(xr[2][0] + 36864); B[7][1] = *(LAS bf16x8*)(xr[3][0] + 36864); SB();
    MF1(0, 0, A[5][0], B[5][0]); SB();
    A[0][1] = tr_read2(wr[1][0] + 0, 1024); LOAD_ND(); SB();
    MF1(0, 0, A[6][0], B[6][0]); SB();
    A[1][1] = tr_read2(wr[1][0] + 4096, 1024); SB();
    MF1(0, 0, A[7][0], B[7][0]); SB();
    A[2][1] = tr_read2(wr[1][0] + 8192, 1024); SB();
    MF1(0, 1, A[4][0], B[4][1]); SB();
    A[3][1] = tr_read2(wr[1][0] + 12288, 1024); SB();
    MF1(0, 1, A[5][0], B[5][1]); SB();
    A[4][1] = tr_read2(wr[1][0] + 32768, 1024); EPI_WORD(0, 0, 0, 0); SB();
    MF1(0, 1, A[6][0], B[6][1]); SB();
    A[5][1] = tr_read2(wr[1][0] + 36864, 1024); EPI_WORD(0, 0, 0, 1); SB();
    MF1(0, 1, A[7][0], B[7][1]); SB();
    A[6][1] = tr_read2(wr[1][0] + 40960, 1024); EPI_WORD(0, 0, 0, 2); SB();
    MF1(1, 1, A[0][1], B[0][1]); SB();
    A[7][1] = tr_read2(wr[1][0] + 45056, 1024); EPI_WORD(0, 0, 0, 3); SB();
    MF1(1, 1, A[1][1], B[1][1]); SB();
    SB(); EPI_STORE(0, 0, 0); SB(); EPI_WORD(0, 0, 1, 0); EPI_WORD(0, 0, 1, 1); SB();
    MF1(1, 1, A[2][1], B[2][1]); SB();
    EPI_WORD(0, 0, 1, 2); EPI_WORD(0, 0, 1, 3); SB();
    MF1(1, 1, A[3][1], B[3][1]); SB();
    SB(); EPI_STORE(0, 0, 1); SB(); EPI_WORD(0, 1, 0, 0); EPI_WORD(0, 1, 0, 1); SB();
    MF1(1, 1, A[4][1], B[4][1]); SB();
    EPI_WORD(0, 1, 0, 2); EPI_WORD(0, 1, 0, 3); SB();
    MF1(1, 1, A[5][1], B[5][1]); SB();
    SB(); EPI_STORE(0, 1, 0); SB(); EPI_WORD(0, 1, 1, 0); EPI_WORD(0, 1, 1, 1); SB();
    MF1(1, 1, A[6][1], B[6][1]); SB();
    WAIT_LGKM0(); BARRIER(); SB();
    MF1(1, 1, A[7][1], B[7][1]); SB();
    EPI_WORD(0, 1, 1, 2); EPI_WORD(0, 1, 1, 3); SB();
    MF1(1, 0, A[0][1], B[0][0]); SB();
    SB(); EPI_STORE(0, 1, 1); SB(); dma_n<1024>(v2[1], rs_w2, s2o[2] + 32 * W2ROW); SB();
    MF1(1, 0, A[1][1], B[1][0]); SB();
    EPI_WORD(1, 1, 0, 0); EPI_WORD(1, 1, 0, 1); SB();
    MF1(1, 0, A[2][1], B[2][0]); SB();
    EPI_WORD(1, 1, 0, 2); EPI_WORD(1, 1, 0, 3); SB();
    MF1(1, 0, A[3][1], B[3][0]); SB();
    SB(); EPI_STORE(1, 1, 0); SB(); EPI_WORD(1, 1, 1, 0); EPI_WORD(1, 1, 1, 1); SB();
    MF1(1, 0, A[4][1], B[4][0]); SB();
    EPI_WORD(1, 1, 1, 2); EPI_WORD(1, 1, 1, 3); SB();
    MF1(1, 0, A[5][1], B[5][0]); SB();
    SB(); EPI_STORE(1, 1, 1); SB(); dma_n<2048>(v2[2], rs_w2, s2o[2] + 32 * W2ROW); SB();
    MF1(1, 0, A[6][1], B[6][0]); SB();
    dma_n<3072>(v2[3], rs_w2, s2o[2] + 32 * W2ROW); SB();
    MF1(1, 0, A[7][1], B[7][0]); SB();
    dma_m<0>(m0w2 + 0, v2[0], rs_w2, s2o[3] + 0 * W2ROW); SB();
    asm volatile("s_nop 15" ::: "memory"); SB();
    EPI_WORD(1, 0, 0, 0); EPI_WORD(1, 0, 0, 1); EPI_WORD(1, 0, 0, 2); EPI_WORD(1, 0, 0, 3); SB(); EPI_STORE(1, 0, 0); SB();
    EPI_WORD(1, 0, 1, 0); EPI_WORD(1, 0, 1, 1); EPI_WORD(1, 0, 1, 2); EPI_WORD(1, 0, 1, 3); SB(); EPI_STORE(1, 0, 1); SB();
    dma_n<1024>(v2[1], rs_w2, s2o[3] + 0 * W2ROW);
    dma_n<2048>(v2[2], rs_w2, s2o[3] + 0 * W2ROW);
    dma_n<3072>(v2[3], rs_w2, s2o[3] + 0 * W2ROW);
  } else {
    WAIT_VM(0);
#pragma unroll
    for (int i = 0; i < 6; ++i) asm volatile("" :: "v"(cpf[i]));
    PICK_NONCE();
    {
      const auto rx = __builtin_amdgcn_make_buffer_rsrc((void*)x, 0, X_BYTES, 0x00020000);
      const auto r1 = __builtin_amdgcn_make_buffer_rsrc((void*)w1, 0, W1_BYTES, 0x00020000);
      const auto r2 = __builtin_amdgcn_make_buffer_rsrc((void*)w2, 0, W2_BYTES, 0x00020000);
      u32x4 t = {0, 0, 0, 0};
      const unsigned q4 = idx >> 3;
      {
        const unsigned xo = (unsigned)(grp * 512 + (par * 4 + q4) * 64 + wave * 16) * XROW + 640;
#pragma unroll
        for (int i = 0; i < 10; ++i) { const unsigned c = i * 64 + lane; t ^= __builtin_amdgcn_raw_buffer_load_b128(rx, (c / 40) * XROW + (c % 40) * 16, xo, 0); }
        const unsigned wo = (unsigned)(320 + (grp * 4 + q4) * 20 + wave * 5) * W1ROW + par * 1664;
#pragma unroll
        for (int i = 0; i < 9; ++i) { unsigned c = i * 64 + lane; c = c < 560 ? c : 559; t ^= __builtin_amdgcn_raw_buffer_load_b128(r1, (c / 112) * W1ROW + (c % 112) * 16, wo, 0); }
      }
      {
        const unsigned wo2 = (unsigned)((grp * 4 + q4) * 108 + wave * 27) * W2ROW + par * 640;
#pragma unroll
        for (int i = 0; i < 17; ++i) { unsigned c = i * 64 + lane; c = c < 1080 ? c : 1079; t ^= __builtin_amdgcn_raw_buffer_load_b128(r2, (c / 40) * W2ROW + (c % 40) * 16, wo2, 0); }
      }
      asm volatile("" :: "v"(t));
    }
    WAIT_VM(0);
    if (wave == PW) st_agent(BOX_WR, ld_agent(nonce_w + lane));
    dma_m<0>(m0w2 + 131072, v2[0], rs_w2, s2o[0] + 0 * W2ROW);
    dma_n<1024>(v2[1], rs_w2, s2o[0] + 0 * W2ROW);
    dma_n<2048>(v2[2], rs_w2, s2o[0] + 0 * W2ROW);
    dma_n<3072>(v2[3], rs_w2, s2o[0] + 0 * W2ROW);
    dma_m<0>(m0w2 + 135168, v2[0], rs_w2, s2o[0] + 32 * W2ROW);
    dma_n<1024>(v2[1], rs_w2, s2o[0] + 32 * W2ROW);
    dma_n<2048>(v2[2], rs_w2, s2o[0] + 32 * W2ROW);
    dma_n<3072>(v2[3], rs_w2, s2o[0] + 32 * W2ROW);
    dma_m<0>(m0w2 + 65536, v2[0], rs_w2, s2o[1] + 0 * W2ROW);
    dma_n<1024>(v2[1], rs_w2, s2o[1] + 0 * W2ROW);
    dma_n<2048>(v2[2], rs_w2, s2o[1] + 0 * W2ROW);
    dma_n<3072>(v2[3], rs_w2, s2o[1] + 0 * W2ROW);
    dma_m<0>(m0w2 + 69632, v2[0], rs_w2, s2o[1] + 32 * W2ROW);
    dma_n<1024>(v2[1], rs_w2, s2o[1] + 32 * W2ROW);
    dma_n<2048>(v2[2], rs_w2, s2o[1] + 32 * W2ROW);
    dma_n<3072>(v2[3], rs_w2, s2o[1] + 32 * W2ROW);
    dma_m<0>(m0w2 + 98304, v2[0], rs_w2, s2o[2] + 0 * W2ROW);
    dma_n<1024>(v2[1], rs_w2, s2o[2] + 0 * W2ROW);
    dma_n<2048>(v2[2], rs_w2, s2o[2] + 0 * W2ROW);
    dma_n<3072>(v2[3], rs_w2, s2o[2] + 0 * W2ROW);
    dma_m<0>(m0w2 + 102400, v2[0], rs_w2, s2o[2] + 32 * W2ROW);
    dma_n<1024>(v2[1], rs_w2, s2o[2] + 32 * W2ROW);
    dma_n<2048>(v2[2], rs_w2, s2o[2] + 32 * W2ROW);
    dma_n<3072>(v2[3], rs_w2, s2o[2] + 32 * W2ROW);
    dma_m<0>(m0w2 + 0, v2[0], rs_w2, s2o[3] + 0 * W2ROW);
    dma_n<1024>(v2[1], rs_w2, s2o[3] + 0 * W2ROW);
    dma_n<2048>(v2[2], rs_w2, s2o[3] + 0 * W2ROW);
    dma_n<3072>(v2[3], rs_w2, s2o[3] + 0 * W2ROW);
    WAIT_VM(28);
    LOAD_ND();
    WAIT_VM(0);
  }

  bf16x8 W[14][4];
  unsigned nd2 = 0;
  {
    const int g = lane >> 4, q = (lane & 15) >> 2, p = lane & 3;
    const unsigned f = ((q >> 1) & 1) | ((g & 1) << 1);
    const unsigned ra = wave * 8192 + (16 * (g & 1) + 4 * (g >> 1) + q) * 128 + ((p >> 1) << 4) + 8 * (p & 1);
    LAS char* wb[4];
#pragma unroll
    for (int nb = 0; nb < 4; ++nb) wb[nb] = smem + ra + ((nb ^ f) << 5);
#define RW(j, k, nb) W[2 * (j) + (k)][nb] = tr_read2(wb[nb] + W2SLOT(j) + (k) * 4096, 1024)
    const auto rs_h = __builtin_amdgcn_make_buffer_rsrc((void*)ws, 0, H_BYTES, 0x00020000);
    const unsigned hb = ((unsigned)mb0 * KS2 + wave * 14) * 1024;
    const unsigned vo = lane * 16;
    constexpr int HD = HDIST, HR = HDIST + 1;
    u32x4 H[HR];
    auto load_h1 = [&](int t) {
      const int mb = t / 14, k = t % 14;
      const unsigned so = mb < nmb ? hb + (unsigned)mb * (KS2 * 1024) : 0x40000000u;
      H[t % HR] = __builtin_amdgcn_raw_buffer_load_b128(rs_h, vo + (k & 1) * 1024, so + hko[k >> 1], 16);
    };
    unsigned pv;
#define POLL_ISSUE() do { pv = nonce; if (wave == PW) LD_ASYNC(pv, BOX_RD); } while (0)
#define POLL_WAIT() do { asm volatile("" : "+v"(pv)); \
      for (unsigned spins = 0; __builtin_amdgcn_ballot_w64(pv != nonce) != 0 && spins < (1u << 16); ++spins) { \
        unsigned n_; \
        asm volatile("global_load_dword %0, %2, off sc1\n\tglobal_load_dword %1, %3, off sc1\n\ts_waitcnt vmcnt(0)" \
                     : "=&v"(pv), "=&v"(n_) : "v"(BOX_RD), "v"(nonce_w + lane) : "memory"); \
        if (__builtin_amdgcn_ballot_w64(n_ != nd) != 0) { nd = n_; st_agent(BOX_WR, nd); } } \
      BARRIER();        \
      if (wave == PW) nd2 = ld_agent(nonce_w + lane);     \
      _Pragma("unroll") for (int t = 0; t < HD; ++t) load_h1(t); \
      SB(); } while (0)
    dma_m<0>(m0w2 + 4096, v2[0], rs_w2, s2o[3] + 32 * W2ROW); WAIT_VM(29); RW(0, 0, 0); RW(0, 0, 1); SB();
    dma_n<1024>(v2[1], rs_w2, s2o[3] + 32 * W2ROW); RW(0, 0, 2); RW(0, 0, 3); SB();
    dma_n<2048>(v2[2], rs_w2, s2o[3] + 32 * W2ROW); RW(0, 1, 0); RW(0, 1, 1); SB();
    dma_n<3072>(v2[3], rs_w2, s2o[3] + 32 * W2ROW); RW(0, 1, 2); RW(0, 1, 3); SB();
    WAIT_VM(0);
    asm volatile("" : "+v"(nd));
    BARRIER();
    if (wave == PW) st_agent(BOX_WR, nd);
    dma_m<0>(m0w2 + 32768, v2[0], rs_w2, s2o[4] + 0 * W2ROW); WAIT_VM(25); RW(1, 0, 0); RW(1, 0, 1); SB();
    dma_n<1024>(v2[1], rs_w2, s2o[4] + 0 * W2ROW); RW(1, 0, 2); RW(1, 0, 3); SB();
    dma_n<2048>(v2[2], rs_w2, s2o[4] + 0 * W2ROW); RW(1, 1, 0); RW(1, 1, 1); SB();
    dma_n<3072>(v2[3], rs_w2, s2o[4] + 0 * W2ROW); RW(1, 1, 2); RW(1, 1, 3); SB();
    dma_m<0>(m0w2 + 36864, v2[0], rs_w2, s2o[4] + 32 * W2ROW); WAIT_VM(15); RW(2, 0, 0); RW(2, 0, 1); SB();
    dma_n<1024>(v2[1], rs_w2, s2o[4] + 32 * W2ROW); RW(2, 0, 2); RW(2, 0, 3); SB();
    dma_n<2048>(v2[2], rs_w2, s2o[4] + 32 * W2ROW); RW(2, 1, 0); RW(2, 1, 1); SB();
    dma_n<3072>(v2[3], rs_w2, s2o[4] + 32 * W2ROW); RW(2, 1, 2); RW(2, 1, 3); SB();
    WAIT_LGKM0();
    dma_m<0>(m0w2 + 131072, v2[0], rs_w2, s2o[5] + 0 * W2ROW); WAIT_VM(9); RW(3, 0, 0); RW(3, 0, 1); SB();
    dma_n<1024>(v2[1], rs_w2, s2o[5] + 0 * W2ROW); RW(3, 0, 2); RW(3, 0, 3); SB();
    dma_n<2048>(v2[2], rs_w2, s2o[5] + 0 * W2ROW); RW(3, 1, 0); RW(3, 1, 1); SB();
    dma_n<3072>(v2[3], rs_w2, s2o[5] + 0 * W2ROW); RW(3, 1, 2); RW(3, 1, 3); SB();
    dma_m<0>(m0w2 + 135168, v2[0], rs_w2, s2o[5] + 32 * W2ROW);  SB();
    dma_n<1024>(v2[1], rs_w2, s2o[5] + 32 * W2ROW);  SB();
    dma_n<2048>(v2[2], rs_w2, s2o[5] + 32 * W2ROW);  SB();
    dma_n<3072>(v2[3], rs_w2, s2o[5] + 32 * W2ROW); WAIT_VM(8); RW(4, 0, 0); RW(4, 0, 1); SB();
    POLL_ISSUE();
    WAIT_LGKM0();
    dma_m<0>(m0w2 + 65536, v2[0], rs_w2, s2o[6] + 0 * W2ROW); RW(4, 0, 2); RW(4, 0, 3); SB();
    dma_n<1024>(v2[1], rs_w2, s2o[6] + 0 * W2ROW); RW(4, 1, 0); RW(4, 1, 1); SB();
    dma_n<2048>(v2[2], rs_w2, s2o[6] + 0 * W2ROW); RW(4, 1, 2); RW(4, 1, 3); SB();
    dma_n<3072>(v2[3], rs_w2, s2o[6] + 0 * W2ROW);  SB();
    dma_m<0>(m0w2 + 69632, v2[0], rs_w2, s2o[6] + 32 * W2ROW);  SB();
    dma_n<1024>(v2[1], rs_w2, s2o[6] + 32 * W2ROW);  SB();
    dma_n<2048>(v2[2], rs_w2, s2o[6] + 32 * W2ROW);  SB();
    dma_n<3072>(v2[3], rs_w2, s2o[6] + 32 * W2ROW); WAIT_VM(8); RW(5, 0, 0); RW(5, 0, 1); SB();
    WAIT_VM(8);
    POLL_WAIT();
    RW(5, 0, 2); RW(5, 0, 3); RW(5, 1, 0); RW(5, 1, 1); RW(5, 1, 2); RW(5, 1, 3); WAIT_VM(12); RW(6, 0, 0); RW(6, 0, 1); SB();
    RW(6, 0, 2); RW(6, 0, 3); RW(6, 1, 0); RW(6, 1, 1); RW(6, 1, 2); RW(6, 1, 3); SB();
    WAIT_LGKM0(); BARRIER();
    const int m = lane & 15;
    const auto rs_o = __builtin_amdgcn_make_buffer_rsrc((void*)out, 0, OUT_BYTES, 0x00020000);
    const int c = lane & 7, mr = (wave & 1) * 8 + (lane >> 3);
    LAS char* rbase = smem + (wave >> 1) * 16384 + (c >> 1) * 1024 + (c & 1) * 512 + ((mr ^ (2 * c)) << 4);
    const unsigned ovo = (unsigned)((mb0 + (wave >> 1)) * 16 + mr) * (kD * 2) + c * 16;
    f32x4 rp[8];
    auto red_read = [&](int pr) {
#pragma unroll
      for (int wv = 0; wv < 4; ++wv) { rp[2 * wv] = *(LAS f32x4*)(rbase + pr * 32768 + wv * 4096); rp[2 * wv + 1] = *(LAS f32x4*)(rbase + pr * 32768 + wv * 4096 + 256); }
    };
    auto red_store = [&](int pr) {
      const f32x4 lo = (rp[0] + rp[2]) + (rp[4] + rp[6]), hi4 = (rp[1] + rp[3]) + (rp[5] + rp[7]);
      u32x4 v;
      v[0] = pack_bf16(lo[0], lo[1]); v[1] = pack_bf16(lo[2], lo[3]); v[2] = pack_bf16(hi4[0], hi4[1]); v[3] = pack_bf16(hi4[2], hi4[3]);
      const unsigned so = (2 * pr + (wave >> 1) < nmb) ? lc2 * 128 + pr * (32 * kD * 2) : 0x80000000u;
      __builtin_amdgcn_raw_buffer_store_b128(v, rs_o, ovo, so, 16);
      SB(); asm volatile("s_nop 1");
    };
#pragma unroll
    for (int mb = 0; mb < 6; ++mb) {
      if (mb < 4 || mb < nmb) {
        f32x4 acc[4];
#pragma unroll
        for (int nb = 0; nb < 4; ++nb) acc[nb] = f32x4{0.f, 0.f, 0.f, 0.f};
#pragma unroll
        for (int k = 0; k < 14; ++k) {
          const int t = mb * 14 + k;
#pragma unroll
          for (int nb = 0; nb < 4; ++nb) {
            acc[nb] = __builtin_amdgcn_mfma_f32_16x16x32_bf16(W[k][nb], __builtin_bit_cast(bf16x8, H[t % HR]), acc[nb], 0, 0, 0);
            if (nb == 0 && t + HD < 84) { SB(); load_h1(t + HD); SB(); }
            if ((mb == 2 || mb == 4) && nb == 2) {
              if (k == 1) { SB(); WAIT_LGKM0(); BARRIER(); SB(); }
              if (k == 2) { SB(); red_read(mb / 2 - 1); SB(); }
              if (k == 6) { SB(); red_store(mb / 2 - 1); SB(); }
            }
          }
        }
#pragma unroll
        for (int nb = 0; nb < 4; ++nb)
          *(LAS f32x4*)(smem + mb * 16384 + wave * 4096 + nb * 1024 + g * 256 + ((m ^ (4 * nb + (g & 2))) << 4)) = acc[nb];
        SB();
      }
    }
    WAIT_LGKM0();
    BARRIER();
    if (nmb > 4) { red_read(2); red_store(2); } else { red_read(1); red_store(1); }
  }
  if (wave == PW && __builtin_amdgcn_ballot_w64(nd2 != nd) != 0) st_agent(BOX_WR, nd2);
  asm volatile(".Lffn_kend:");
}

extern "C" void kernel_launch(void* const* d_in, const int* in_sizes, int n_in, void* d_out, int out_size, void* d_ws,
                              size_t ws_size, hipStream_t stream) {
  (void)in_sizes; (void)n_in; (void)out_size; (void)ws_size;
  (void)hipFuncSetAttribute((const void*)ffn_relu_kernel, hipFuncAttributeMaxDynamicSharedMemorySize, LDS_BYTES);
  ffn_relu_kernel<<<256, 256, LDS_BYTES, stream>>>((const char*)d_in[0], (const char*)d_in[1], (const char*)d_in[2],
                                                   (char*)d_out, (char*)d_ws);
}
```
